```python
import torch

def _softmax(x, axis=-1):
    return torch.softmax(x, dim=axis)
def _gelu(x):
    return torch.nn.functional.gelu(x, approximate="tanh")
def _silu(x):
    return torch.nn.functional.silu(x)
def _sigmoid(x):
    return torch.sigmoid(x)
def _layernorm(x, axis=-1, eps=1e-5):
    m = x.mean(dim=axis, keepdim=True)
    v = x.var(dim=axis, keepdim=True, unbiased=False)
    return (x - m) / torch.sqrt(v + eps)
def _scatter_add(tgt, idx, src, axis=0):
    return tgt.clone().index_add_(axis, idx, src)
def _segment_sum(src, seg, n):
    out = torch.zeros((n,) + src.shape[1:], dtype=src.dtype)
    return out.index_add_(0, seg, src)
def _pad(x, pw):
    flat = tuple(v for pair in reversed(pw) for v in pair)
    return torch.nn.functional.pad(x, flat)

kernel_name = 'sequential_preLN__compressed_kv_cla__swiglu_ffn__prompt'

def setup_inputs():

    _w = (lambda a: (lambda t: t.to(torch.bfloat16) if t.is_floating_point() else t)(torch.as_tensor(a)))
    def _A_setup():
        B, H, Sq, Sk, D, M = 1, 32, 2048, 2048, 128, 576
        Dc = 4 * D
        return {
            "x": _w((lambda *s: torch.randn(*s, dtype=torch.bfloat16) * 0.05)(B, Sq, M)),
            "c_kv": _w((lambda *s: torch.randn(*s, dtype=torch.bfloat16) * 0.4)(B, Sk, Dc)),
            "w_q": _w((lambda *s: torch.randn(*s, dtype=torch.bfloat16) * 1.6)(M, H, D) * (M ** -0.5)),
            "w_uk": _w((lambda *s: torch.randn(*s, dtype=torch.bfloat16) * 1.6)(Dc, H, D) * (Dc ** -0.5)),
            "w_uv": _w((lambda *s: torch.randn(*s, dtype=torch.bfloat16) * 0.4)(Dc, H, D) * (Dc ** -0.5)),
            "w_o": _w((lambda *s: torch.randn(*s, dtype=torch.bfloat16) * 0.4)(H, D, M) * ((H * D) ** -0.5)),
        }
    _A_in = _A_setup()

    def _B_setup():
        B, S, D, F = 1, 2048, 576, 4608
        return {
            "x": _w((lambda *s: torch.randn(*s, dtype=torch.bfloat16) * 0.05)(B, S, D)),
            "w_gate": _w((lambda *s: torch.randn(*s, dtype=torch.bfloat16) * 0.05)(D, F)),
            "w_up": _w((lambda *s: torch.randn(*s, dtype=torch.bfloat16) * 0.05)(D, F)),
            "w_down": _w((lambda *s: torch.randn(*s, dtype=torch.bfloat16) * 0.05)(F, D)),
        }
    _B_in = _B_setup()

    return {
        "A_x": _A_in["x"],
        "A_c_kv": _A_in["c_kv"],
        "A_w_q": _A_in["w_q"],
        "A_w_uk": _A_in["w_uk"],
        "A_w_uv": _A_in["w_uv"],
        "A_w_o": _A_in["w_o"],
        "B_x": _B_in["x"],
        "B_w_gate": _B_in["w_gate"],
        "B_w_up": _B_in["w_up"],
        "B_w_down": _B_in["w_down"],
    }

def reference(A_x, A_c_kv, A_w_q, A_w_uk, A_w_uv, A_w_o, B_x, B_w_gate, B_w_up, B_w_down):
    def _A_ref(x, c_kv, w_q, w_uk, w_uv, w_o):
        B, Sq, M = x.shape[0], x.shape[1], x.shape[2]
        Sk = c_kv.shape[1]
        H, D = w_q.shape[1], w_q.shape[2]
        q = torch.einsum("bqm,mhd->bhqd", x, w_q)
        k = torch.einsum("bkc,chd->bhkd", c_kv, w_uk)
        v = torch.einsum("bkc,chd->bhkd", c_kv, w_uv)
        scores = torch.einsum("bhqd,bhkd->bhqk", q, k) * (D ** -0.5)
        off = Sk - Sq
        qi = torch.arange(Sq)[:, None]
        kj = torch.arange(Sk)[None, :]
        cmask = kj <= (qi + off)
        scores = torch.where(cmask[None, None], scores, -1e9)
        p = _softmax(scores, axis=-1)
        ctx = torch.einsum("bhqk,bhkd->bhqd", p, v)
        return torch.einsum("bhqd,hdm->bqm", ctx, w_o)

    def _B_ref(x, w_gate, w_up, w_down):
        g = _silu(x @ w_gate)
        u = x @ w_up
        return (g * u) @ w_down

    x = A_x
    h = x + _A_ref(_layernorm(x, axis=-1), A_c_kv, A_w_q, A_w_uk, A_w_uv, A_w_o)
    h2 = h + _B_ref(_layernorm(h, axis=-1), B_w_gate, B_w_up, B_w_down)
    return h2
```

```cpp
#include <hip/hip_runtime.h>
#include <stdint.h>
#include <utility>

#define LAS __attribute__((address_space(3)))
#define DI __device__ __forceinline__
typedef unsigned u32x4 __attribute__((ext_vector_type(4)));
typedef unsigned u32x2 __attribute__((ext_vector_type(2)));
typedef short bf16x8 __attribute__((ext_vector_type(8)));
typedef short s16x4 __attribute__((ext_vector_type(4)));
typedef float f32x4 __attribute__((ext_vector_type(4)));
typedef LAS const char* lds_cptr;
typedef __amdgpu_buffer_rsrc_t rsrc_t;
template <int... Is> using iseq = std::integer_sequence<int, Is...>;
template <int N> using mkseq = std::make_integer_sequence<int, N>;
#define SBAR() __builtin_amdgcn_sched_barrier(0)

constexpr int S_ = 2048, DM = 576, NH = 32, HD = 128, DC = 512, FF = 4608;
constexpr size_t SZ_XN = (size_t)128 * 18 * 1024, SZ_KF = (size_t)32 * 128 * 4 * 1024, SZ_VF = SZ_KF, SZ_CTX = (size_t)128 * 128 * 1024, SZ_HN = SZ_XN, SZ_ACT = (size_t)128 * 144 * 1024;
constexpr size_t SZ_PART = (size_t)128 * 36 * 4 * 1024, SZ_H = (size_t)128 * 36 * 1024, SZ_QS = (size_t)256 * 4 * 16384;
constexpr size_t OFF_XN = 0, OFF_KF = OFF_XN + SZ_XN, OFF_VF = OFF_KF + SZ_KF, OFF_CTX = OFF_VF + SZ_VF, OFF_HN = OFF_CTX + SZ_CTX, OFF_ACT = OFF_HN + SZ_HN;
constexpr size_t OFF_P1 = OFF_ACT + SZ_ACT, OFF_P2 = OFF_P1 + SZ_PART, OFF_H = OFF_P2 + SZ_PART, OFF_QS = OFF_H + SZ_H, OFF_END = OFF_QS + SZ_QS;

#define STAMP_DECL
#define STAMP_AT(c)
#define STAMP_OUT(c, ws, kid)
DI rsrc_t mkrsrc(const void* p, unsigned bytes) { return __builtin_amdgcn_make_buffer_rsrc((void*)p, (short)0, (int)bytes, 0x00020000); }
DI unsigned rfl(unsigned x) { return __builtin_amdgcn_readfirstlane(x); }
DI s16x4 vtr(lds_cptr p) { return __builtin_amdgcn_ds_read_tr16_b64_v4i16((LAS s16x4*)p); }
DI bf16x8 cat(s16x4 a, s16x4 b) { return __builtin_shufflevector(a, b, 0, 1, 2, 3, 4, 5, 6, 7); }
typedef float f32x2 __attribute__((ext_vector_type(2)));
typedef __bf16 bf16x2_t __attribute__((ext_vector_type(2)));
DI unsigned cvtpk(float lo, float hi) { f32x2 v = {lo, hi}; return __builtin_bit_cast(unsigned, __builtin_convertvector(v, bf16x2_t)); }
template <int N> DI void wait_vm() { asm volatile("s_waitcnt vmcnt(%0)" ::"i"(N > 63 ? 63 : N) : "memory"); }
template <int N> DI void wait_vm_bar() { asm volatile("s_waitcnt vmcnt(%0)\n\ts_barrier" ::"i"(N > 63 ? 63 : N) : "memory"); }
DI void wait_lgkm0() { asm volatile("s_waitcnt lgkmcnt(0)" ::: "memory"); }
DI void dma16(unsigned m0v, unsigned voff, rsrc_t rs, unsigned soff) {
    asm volatile("s_mov_b32 m0, %0\n\ts_nop 0\n\tbuffer_load_dwordx4 %1, %2, %3 offen lds" ::"s"(m0v), "v"(voff), "s"(rs), "s"(soff) : "m0", "memory");
}
template <bool Z> DI void mf_va(f32x4& c, const bf16x8& w, const bf16x8& a) {
    if constexpr (Z) asm volatile("v_mfma_f32_16x16x32_bf16 %0, %1, %2, 0" : "=&v"(c) : "v"(w), "a"(a));
    else asm volatile("v_mfma_f32_16x16x32_bf16 %0, %1, %2, %0" : "+v"(c) : "v"(w), "a"(a));
}
template <bool Z> DI void mf_av(f32x4& c, const bf16x8& a, const bf16x8& w) {
    if constexpr (Z) asm volatile("v_mfma_f32_16x16x32_bf16 %0, %1, %2, 0" : "=&v"(c) : "a"(a), "v"(w));
    else asm volatile("v_mfma_f32_16x16x32_bf16 %0, %1, %2, %0" : "+v"(c) : "a"(a), "v"(w));
}
template <bool Z> DI void mf_vv(f32x4& c, const bf16x8& w, const bf16x8& a) {
    if constexpr (Z) asm volatile("v_mfma_f32_16x16x32_bf16 %0, %1, %2, 0" : "=&v"(c) : "v"(w), "v"(a));
    else asm volatile("v_mfma_f32_16x16x32_bf16 %0, %1, %2, %0" : "+v"(c) : "v"(w), "v"(a));
}
DI void ldfrag_a(bf16x8& d, unsigned voff, rsrc_t rs, unsigned soff) { asm volatile("buffer_load_dwordx4 %0, %1, %2, %3 offen" : "=a"(d) : "v"(voff), "s"(rs), "s"(soff) : "memory"); }
DI void ldfrag_v(bf16x8& d, unsigned voff, rsrc_t rs, unsigned soff) { asm volatile("buffer_load_dwordx4 %0, %1, %2, %3 offen" : "=v"(d) : "v"(voff), "s"(rs), "s"(soff) : "memory"); }
DI float acc_rd(float a) { float t; asm volatile("v_accvgpr_read_b32 %0, %1" : "=v"(t) : "a"(a)); return t; }
DI float acc_wr(float v) { float a; asm volatile("v_accvgpr_write_b32 %0, %1" : "=a"(a) : "v"(v)); return a; }
DI f32x4 acc_rd4(const f32x4& a) { return f32x4{acc_rd(a[0]), acc_rd(a[1]), acc_rd(a[2]), acc_rd(a[3])}; }
DI void acc_scale(f32x4& a, float f) { const f32x4 t = acc_rd4(a); a = f32x4{acc_wr(t[0] * f), acc_wr(t[1] * f), acc_wr(t[2] * f), acc_wr(t[3] * f)}; asm volatile("" : "+a"(a)); }
DI void barrier_lds() { asm volatile("s_waitcnt lgkmcnt(0)\n\ts_barrier" ::: "memory"); }
#define STORE_AUX 0
template <int CTRL> DI float dpp(float v) { return __builtin_bit_cast(float, __builtin_amdgcn_update_dpp(0, __builtin_bit_cast(int, v), CTRL, 0xf, 0xf, true)); }
DI float sum32(float s) {
    s += dpp<0xB1>(s); s += dpp<0x4E>(s); s += dpp<0x141>(s); s += dpp<0x140>(s);
    auto r = __builtin_amdgcn_permlane16_swap(__float_as_uint(s), __float_as_uint(s), false, false);
    return __uint_as_float(r[0]) + __uint_as_float(r[1]);
}
DI float bf2f(unsigned short b) { return __uint_as_float((unsigned)b << 16); }

enum : int { OP_RD = 1, OP_DMA, OP_ALD, OP_EPI, OP_SETB, OP_ADMA, OP_ARD, OP_AWAIT };
constexpr int OPC(int k, int a) { return k * 65536 + a; }
template <class C> struct Eng {
    using Ctx = typename C::Ctx;
    static constexpr int gaps(int st) { return C::snks(st) * C::nt(C::spass(st)) * C::TT; }
    static constexpr int gstart(int st) { int g = 0; for (int i = 0; i < st; ++i) g += gaps(i); return g; }
    static constexpr int NG = gstart(C::NSTG);
    struct Plan { int nop[NG]; int op[NG][6]; int warg[NG]; int waitA[C::NKS]; int waitW[C::NSTG]; int npro; int pro[200]; int left; int waitA0, waitPre; };
    static constexpr Plan mk() {
        Plan p{}; int cnt = C::PRE_VM;
        int posA[C::NKS + 8] = {}, posW[C::NSTG] = {};
        p.npro = C::pro_list(p.pro);
        for (int i = 0; i < p.npro; ++i) {
            const int k = p.pro[i] >> 16, a = p.pro[i] & 65535; ++cnt;
            if (k == OP_DMA) posW[a >> 4] = cnt; else if (k == OP_ALD) posA[a >> 2] = cnt; else if (k == OP_ADMA) posA[a >> 4] = cnt;
        }
        p.waitPre = cnt - C::PRE_VM;
        cnt += C::POST_PRO_VM;
        p.waitA0 = cnt - posA[0];
        int qh[200] = {}, qhn = 0, qhh = 0;
        int qm[200] = {}, qmn = 0, qmh = 0;
        int ql[600] = {}, qln = 0, qlh = 0;
        int G = 0, nexta = C::AD, epi_gap = 0, epi_per = 1;
        for (int st = 0; st < C::NSTG; ++st) {
            const int P = C::spass(st), NT = C::nt(P), GK = NT * C::TT;
            { const int need = (C::cert_next(st) && st + 1 < C::NSTG) ? st + 1 : st; p.waitW[st] = cnt - posW[need]; }
            for (int g = 0; g < C::NSTG; ++g) if (C::wissue(g) == st) for (int i = 0; i < C::ppw(g); ++i) qm[qmn++] = OPC(OP_DMA, g * 16 + i);
            const bool first_of_pass = st == 0 || C::spass(st - 1) != P;
            for (int s = 0; s < C::snks(st); ++s) {
                const int ks = C::sk0(st) + s;
                if (!C::A_VIA_LDS && P == 0) for (int rr = 0; rr < 2; ++rr) if (nexta < C::NKS && nexta <= ks + C::ADX) { for (int t = 0; t < C::TT; ++t) qh[qhn++] = OPC(OP_ALD, nexta * 4 + t); ++nexta; }
                if (!C::A_VIA_LDS && P == 0) p.waitA[ks] = cnt - posA[ks];
                if (C::A_VIA_LDS && P == 0) for (int a = 1; a < C::NKS / 4; ++a) if ((a == 1 ? 0 : (a == 2 ? 1 : 4)) == ks) for (int i = 0; i < 16; ++i) qh[qhn++] = OPC(OP_ADMA, a * 16 + i);
                const bool lastk = s + 1 == C::snks(st);
                const bool hasnext = !lastk || (st + 1 < C::NSTG && C::cert_next(st));
                const int NTn = lastk ? (st + 1 < C::NSTG ? C::nt(C::spass(st + 1)) : 0) : NT;
                const int nrd = hasnext ? 2 * NTn : 0;
                for (int m = 0; m < GK; ++m, ++G) {
                    int cap = 2;
                    if (m % C::TT == 0 && (C::TT < 4 || m == 0)) cap -= 1;
                    if (P == 0 && m == 0 && !C::A_VIA_LDS) cap -= 1;
                    int n = 0;
                    for (int r = 0; r < nrd; ++r) { const int at = nrd <= GK ? (C::TT == 4 ? r : r * GK / nrd) : r * GK / nrd; if (at == m) { p.op[G][n++] = OPC(OP_RD, r); cap -= 1; } }
                    if (C::A_VIA_LDS && P == 0 && ks + 1 < C::NKS && (ks + 1) % 4 == 0) {
                        if (m == 0) { p.op[G][n++] = OPC(OP_AWAIT, (ks + 1) / 4); p.warg[G] = cnt - posA[(ks + 1) / 4]; cap -= 1; }
                        p.op[G][n++] = OPC(OP_ARD, (ks + 1 + (m >> 2)) * 4 + (m & 3)); cap -= 1;
                    }
                    if (s == C::snks(st) - 2 && st + 1 < C::NSTG && m >= GK - C::nb(st + 1)) { p.op[G][n++] = OPC(OP_SETB, (st + 1) * 4 + (m - (GK - C::nb(st + 1)))); cap -= 1; }
                    if (first_of_pass && P >= 1 && s == 0 && m == (GK > 8 ? 8 : GK - 1)) { for (int i = 0; i < C::nepi(P - 1); ++i) ql[qln++] = OPC(OP_EPI, (P - 1) * 256 + i);
                        epi_per = (C::NKS * GK * 13 / 16) / C::nepi(P - 1); if (epi_per < 1) epi_per = 1; epi_gap = G; }
                    for (;;) {
                        if (n >= 5) break;
                        if (qmh < qmn && (cap >= 2 || (cap >= 1 && (C::TT < 4 || (C::A_VIA_LDS && P == 0))))) { const int o = qm[qmh++]; p.op[G][n++] = o; cap -= 3; ++cnt; posW[(o & 65535) >> 4] = cnt; continue; }
                        if (qhh < qhn) { const int o = qh[qhh], c = (o >> 16) == OP_ADMA ? 3 : 1; if (cap >= 1) { p.op[G][n++] = o; ++qhh; cap -= c; ++cnt; if ((o >> 16) == OP_ALD) posA[(o & 65535) >> 2] = cnt; else posA[(o & 65535) >> 4] = cnt; continue; } }
                        if (qlh < qln && G >= epi_gap) { const int o = ql[qlh], e = o & 255, c = C::epi_cost((o & 65535) >> 8, e); if (cap >= c || (c > 2 && cap >= 2) || (cap >= 1 && GK < 16)) { p.op[G][n++] = o; ++qlh; cap -= c; cnt += C::epi_vm((o & 65535) >> 8, e); epi_gap += epi_per; continue; } }
                        break;
                    }
                    p.nop[G] = n;
                }
            }
            if (qmh < qmn) p.left += 1000;
        }
        p.left += (qhn - qhh) + (qln - qlh);
        return p;
    }
    static constexpr Plan PL = mk();
    static_assert(PL.left == 0, "planner could not place every operation");

    template <int ST, int S, int O, int WA> static DI void doop(Ctx& c) {
        constexpr int k = O >> 16, a = O & 65535;
        if constexpr (k == OP_RD) { constexpr bool lastk = S + 1 == C::snks(ST); constexpr int ksg = C::kseq(ST, S) + 1;
            C::template readW<(lastk ? ST + 1 : ST), (lastk ? 0 : S + 1)>(c, ksg & 1, a >> 1, a & 1); }
        else if constexpr (k == OP_DMA) C::template dma<(a >> 4), (a & 15)>(c);
        else if constexpr (k == OP_ALD) C::template aload<(a >> 2), (a & 3)>(c);
        else if constexpr (k == OP_EPI) C::template epi<(a >> 8), (a & 255)>(c);
        else if constexpr (k == OP_SETB) C::template setb<(a >> 2), (a & 3)>(c);
        else if constexpr (k == OP_ADMA) C::template adma<(a >> 4), (a & 15)>(c);
        else if constexpr (k == OP_ARD) C::template aread<(a >> 2), (a & 3)>(c);
        else if constexpr (k == OP_AWAIT) wait_vm<WA>();
    }
    template <int ST, int M> static DI void gap(Ctx& c) {
        constexpr int P = C::spass(ST), NT = C::nt(P), GK = NT * C::TT, s = M / GK, m = M % GK, cc = m / C::TT, t = m % C::TT, ks = C::sk0(ST) + s, G = gstart(ST) + M;
        if constexpr (!C::A_VIA_LDS && P == 0 && m == 0) wait_vm<PL.waitA[ks]>();
        if constexpr (C::TT == 4 && m == 0) __builtin_amdgcn_s_waitcnt(0xC07F);
        C::template mfma<P, ks, cc, t>(c, cat(c.w[C::kseq(ST, s) & 1][cc][0], c.w[C::kseq(ST, s) & 1][cc][1]));
        if constexpr (PL.nop[G] > 0) doop<ST, s, PL.op[G][0], PL.warg[G]>(c);
        if constexpr (PL.nop[G] > 1) doop<ST, s, PL.op[G][1], PL.warg[G]>(c);
        if constexpr (PL.nop[G] > 2) doop<ST, s, PL.op[G][2], PL.warg[G]>(c);
        if constexpr (PL.nop[G] > 3) doop<ST, s, PL.op[G][3], PL.warg[G]>(c);
        if constexpr (PL.nop[G] > 4) doop<ST, s, PL.op[G][4], PL.warg[G]>(c);
        SBAR();
    }
    template <int ST, int R> static DI void rd0(Ctx& c) { C::template readW<ST, 0>(c, C::kseq(ST, 0) & 1, R >> 1, R & 1); }
    template <int ST, int... Rs> static DI void rd0s(Ctx& c, iseq<Rs...>) { (rd0<ST, Rs>(c), ...); }
    template <int... Bs> static DI void setb0(Ctx& c, iseq<Bs...>) { (C::template setb<0, Bs>(c), ...); }
    template <int ST, int... Ms> static DI void stage_(Ctx& c, iseq<Ms...>) {
        STAMP_AT(c);
        if constexpr (C::bar_lgkm(ST)) wait_lgkm0();
        wait_vm_bar<PL.waitW[ST]>();
        SBAR();
        if constexpr (ST == 0 || !C::cert_next(ST > 0 ? ST - 1 : 0)) { rd0s<ST>(c, mkseq<2 * C::nt(C::spass(ST))>{}); SBAR(); }
        (gap<ST, Ms>(c), ...);
    }
    template <int... Ss> static DI void stages_(Ctx& c, iseq<Ss...>) { (stage_<Ss>(c, mkseq<gaps(Ss)>{}), ...); }
    template <int... Is> static DI void pro_(Ctx& c, iseq<Is...>) { (doop<0, 0, PL.pro[Is], 0>(c), ...); }
    static DI void prologue(Ctx& c) { pro_(c, mkseq<PL.npro>{}); setb0(c, mkseq<C::nb(0)>{}); SBAR(); }
    static DI void run(Ctx& c) { stages_(c, mkseq<C::NSTG>{}); }
    template <int P, int... Es> static DI void epi_all_(Ctx& c, iseq<Es...>) { (C::template epi<P, Es>(c), ...); }
    template <int P> static DI void epi_all(Ctx& c) { epi_all_<P>(c, mkseq<C::nepi(P)>{}); }
};
template <int MO, int SO> DI void dmac(unsigned mb, unsigned voff, rsrc_t rs, unsigned sb) {
    unsigned tmp;
    asm volatile("s_add_u32 m0, %1, %5\n\ts_add_u32 %0, %4, %6\n\tbuffer_load_dwordx4 %2, %3, %0 offen lds" : "=&s"(tmp) : "s"(mb), "v"(voff), "s"(rs), "s"(sb), "i"(MO), "i"(SO) : "m0", "scc", "memory");
}
template <int IMM, bool AG> DI void ldfr(bf16x8& d, unsigned voff, rsrc_t rs, unsigned soff) {
    if constexpr (AG) asm volatile("buffer_load_dwordx4 %0, %1, %2, %3 offen offset:%4" : "=a"(d) : "v"(voff), "s"(rs), "s"(soff), "i"(IMM) : "memory");
    else asm volatile("buffer_load_dwordx4 %0, %1, %2, %3 offen offset:%4" : "=v"(d) : "v"(voff), "s"(rs), "s"(soff), "i"(IMM) : "memory");
}

struct CfgKV {
    static constexpr int TT = 4, NKS = 16, NSTG = 16, AD = 0, ADX = 0, PRE_VM = 5, POST_PRO_VM = 5; static constexpr bool A_VIA_LDS = true;
    static constexpr int spass(int st) { return st >> 2; }
    static constexpr int sk0(int st) { return 4 * (st & 3); }
    static constexpr int snks(int) { return 4; }
    static constexpr int nt(int) { return 4; }
    static constexpr int kseq(int st, int s) { return st * 4 + s; }
    static constexpr int nb(int) { return 4; }
    static constexpr int ppw(int) { return 4; }
    static constexpr int wslot(int g) { constexpr int t[16] = {8, 9, 8, 9, 0, 1, 2, 3, 4, 5, 6, 7, 8, 9, 0, 1}; return t[g]; }
    static constexpr int wissue(int g) { constexpr int t[16] = {-1, 0, 1, 2, 2, 2, 3, 3, 4, 5, 6, 7, 8, 9, 10, 11}; return t[g]; }
    static constexpr bool cert_next(int st) { return st >= 3; }
    static constexpr bool bar_lgkm(int st) { return st == 2 || st == 3; }
    static constexpr int nepi(int p) { return p < 3 ? 24 + 9 : 24; }
    static constexpr int epi_cost(int, int) { return 2; }
    static constexpr int epi_vm(int, int e) { return e >= 24 || e % 3 == 2; }
    static constexpr int pro_list(int* o) { int n = 0;
        for (int i = 0; i < 16; ++i) o[n++] = OPC(OP_ADMA, i); for (int i = 0; i < 4; ++i) o[n++] = OPC(OP_DMA, i); return n; }
    struct Ctx {
        bf16x8 af[4][16]; f32x4 acc[2][4][4]; s16x4 w[2][4][2]; u32x4 eo;
        lds_cptr rb[4], cb[2][4], ab[4];
        unsigned mA, mW, voffA[4], voffW[2], lane16; rsrc_t rsC, rsWk, rsWv, rsK, rsV; unsigned sA, sW, sK, sV;
        u32x4 pf; unsigned pfo; rsrc_t rsP[5];
        STAMP_DECL
    };
    template <int A, int I> static DI void adma(Ctx& c) { dmac<(A & 1) * 65536 + I * 1024, 4 * I * 1024 + A * 256>(c.mA, c.voffA[I & 3], c.rsC, c.sA); }
    template <int KS, int T> static DI void aread(Ctx& c) { c.af[T][KS] = *(const LAS bf16x8*)(c.ab[KS & 3] + T * 4096); }
    template <int G, int I> static DI void dma(Ctx& c) { dmac<wslot(G) * 16384 + I * 1024, (128 * (G & 3) + 8 * I) * 8192 + ((G >> 2) & 1) * 128>(c.mW, c.voffW[I & 1], G < 8 ? c.rsWk : c.rsWv, c.sW); }
    template <int G, int S> static DI void readW(Ctx& c, int buf, int cc, int half) { c.w[buf][cc][half] = vtr(c.cb[G & 1][cc] + S * 4096 + half * 512); }
    template <int G, int I> static DI void setb(Ctx& c) { c.cb[G & 1][I] = c.rb[I] + wslot(G) * 16384; asm volatile("" : "+v"(c.cb[G & 1][I]));
        if constexpr (G >= 1 && G <= 3) { c.ab[I] += (G & 1) ? 65536 : -65536; asm volatile("" : "+v"(c.ab[I])); } }
    template <int KS, int T> static DI void aload(Ctx&) {}
    template <int P, int KS, int CC, int T> static DI void mfma(Ctx& c, const bf16x8& wf) {
        if constexpr (P < 2) mf_va<KS == 0>(c.acc[P & 1][CC][T], wf, c.af[T][KS]); else mf_av<KS == 0>(c.acc[P & 1][CC][T], c.af[T][KS], wf); }
    template <int P, int I> static DI void epi(Ctx& c) {
        constexpr int E = I / 3, ph = I % 3, a = E >> 1, pr = E & 1;
        if constexpr (I >= 24) {
            constexpr int j = P * 9 + (I - 24), mtx = j / 5, k = j % 5;
            if constexpr (mtx < 5) { if constexpr (k == 5 - 1 || true) asm volatile("buffer_load_dwordx4 %0, %1, %2, 0 offen offset:%3" : "+v"(c.pf) : "v"(c.pfo + (k >> 2) * 4096), "s"(c.rsP[mtx]), "i"((k & 3) * 1024) : "memory"); }
            else asm volatile("buffer_load_dwordx4 %0, %1, %2, 0 offen offset:1024" : "+v"(c.pf) : "v"(c.pfo + 4096), "s"(c.rsP[2 + (j - 25)]) : "memory");
        } else if constexpr (ph < 2) {
            const f32x4 x = P < 2 ? c.acc[P & 1][2 * pr + ph][a] : c.acc[P & 1][a][2 * pr + ph];
            c.eo[2 * ph] = cvtpk(x[0], x[1]); c.eo[2 * ph + 1] = cvtpk(x[2], x[3]); asm volatile("" : "+v"(c.eo));
        } else if constexpr (P < 2) __builtin_amdgcn_raw_buffer_store_b128(c.eo, c.rsK, c.lane16, c.sK + (unsigned)((a * 4 + 2 * (P & 1) + pr) * 1024), STORE_AUX);
        else __builtin_amdgcn_raw_buffer_store_b128(c.eo, c.rsV, c.lane16, c.sV + (unsigned)((pr * 8 + 4 * (P & 1) + a) * 1024), STORE_AUX);
    }
};
template <int... Is> DI void kv_aread0(CfgKV::Ctx& c, iseq<Is...>) { (CfgKV::aread<(Is >> 2), (Is & 3)>(c), ...); }

__global__ __launch_bounds__(256, 1) void k_kv(const unsigned short* __restrict__ x, const unsigned short* __restrict__ ckv, const unsigned short* __restrict__ wuk,
                                               const unsigned short* __restrict__ wuv, char* __restrict__ ws,
                                               const unsigned short* __restrict__ wq, const unsigned short* __restrict__ wo, const unsigned short* __restrict__ wg,
                                               const unsigned short* __restrict__ wu, const unsigned short* __restrict__ wd) {
    using E = Eng<CfgKV>;
    extern __shared__ __attribute__((aligned(16))) char lds[];
    const unsigned tid = threadIdx.x, lane = tid & 63, r = lane & 15, g = lane >> 4, q = (lane & 15) >> 2, p = lane & 3;
    CfgKV::Ctx c;
    STAMP_AT(c);
    const unsigned wid = rfl(tid >> 6), lds0 = (unsigned)(uintptr_t)lds; c.lane16 = lane * 16;
    const unsigned bid = blockIdx.x, xcd = bid & 7, idx = bid >> 3, rt = 4 * (xcd & 1) + (idx & 3), h = 8 * (xcd >> 1) + (idx >> 2);
    const unsigned T0 = rt * 256 + wid * 64;
    c.rsC = mkrsrc(ckv, S_ * DC * 2); c.rsWk = mkrsrc(wuk, DC * 4096 * 2); c.rsWv = mkrsrc(wuv, DC * 4096 * 2);
    c.rsK = mkrsrc(ws + OFF_KF, (unsigned)SZ_KF); c.rsV = mkrsrc(ws + OFF_VF, (unsigned)SZ_VF);
    c.mA = lds0 + wid * 16384; c.mW = lds0 + wid * 4096;
    c.rsP[0] = mkrsrc(wq, DM * 4096 * 2); c.rsP[1] = mkrsrc(wo, DM * 4096 * 2); c.rsP[2] = mkrsrc(wg, DM * FF * 2); c.rsP[3] = mkrsrc(wu, DM * FF * 2); c.rsP[4] = mkrsrc(wd, DM * FF * 2);
    c.pfo = (bid * 4 + wid) * 5184 + lane * 16;
    c.pf = u32x4{0u, 0u, 0u, 0u};
    c.sA = T0 * 1024; c.sW = wid * 32 * 8192 + h * 256;
    c.sK = (h * 128 + T0 / 16) * 4096; c.sV = (h * 64 + T0 / 32) * 8192;
#pragma unroll
    for (int i = 0; i < 4; ++i) c.voffA[i] = (lane >> 4) * 1024 + (((lane & 15) ^ (4 * i + (lane >> 4))) << 4);
#pragma unroll
    for (int i = 0; i < 2; ++i) { const unsigned r8 = lane >> 3, f = ((r8 >> 1) & 1) | (i << 1); c.voffW[i] = r8 * 8192 + (((lane & 7) ^ (f << 1)) << 4); }
    { const unsigned f = ((q >> 1) & 1) | ((g & 1) << 1);
#pragma unroll
      for (int cc = 0; cc < 4; ++cc) c.rb[cc] = (lds_cptr)lds + (8 * g + q) * 128 + (((cc ^ f) << 5) + 8 * p); }
#pragma unroll
    for (int T = 0; T < 4; ++T) c.ab[T] = (lds_cptr)lds + wid * 16384 + r * 256 + (((g ^ r) ^ (4 * T)) << 4);
    const unsigned row = bid * 8 + wid * 2 + (lane >> 5), li = lane & 31;
    u32x2 xv[5];
    { const char* xp = (const char*)x + row * (DM * 2) + li * 8; const char* xp4 = (const char*)x + row * (DM * 2) + (128 + (li & 15)) * 8;
      asm volatile("global_load_dwordx2 %0, %1, off" : "=v"(xv[0]) : "v"(xp) : "memory");
      asm volatile("global_load_dwordx2 %0, %1, off offset:256" : "=v"(xv[1]) : "v"(xp) : "memory");
      asm volatile("global_load_dwordx2 %0, %1, off offset:512" : "=v"(xv[2]) : "v"(xp) : "memory");
      asm volatile("global_load_dwordx2 %0, %1, off offset:768" : "=v"(xv[3]) : "v"(xp) : "memory");
      asm volatile("global_load_dwordx2 %0, %1, off" : "=v"(xv[4]) : "v"(xp4) : "memory"); }
    E::prologue(c);
    asm volatile("s_waitcnt vmcnt(%5)" : "+v"(xv[0]), "+v"(xv[1]), "+v"(xv[2]), "+v"(xv[3]), "+v"(xv[4]) : "i"(E::PL.waitPre) : "memory");
    {
        float v[20]; const bool has4 = li < 16;
#pragma unroll
        for (int m = 0; m < 5; ++m) { v[4 * m] = __uint_as_float(xv[m][0] << 16); v[4 * m + 1] = __uint_as_float(xv[m][0] & 0xffff0000u); v[4 * m + 2] = __uint_as_float(xv[m][1] << 16); v[4 * m + 3] = __uint_as_float(xv[m][1] & 0xffff0000u); }
        float s = 0;
#pragma unroll
        for (int e = 0; e < 16; ++e) s += v[e];
        if (has4) s += (v[16] + v[17]) + (v[18] + v[19]);
        s = sum32(s);
        const float mean = s * (1.0f / DM);
        float qq = 0;
#pragma unroll
        for (int e = 0; e < 20; ++e) { v[e] -= mean; if (e < 16 || has4) qq += v[e] * v[e]; }
        qq = sum32(qq);
        const float rstd = __builtin_amdgcn_rsqf(qq * (1.0f / DM) + 1e-5f);
        rsrc_t rsX = mkrsrc(ws + OFF_XN, (unsigned)SZ_XN);
#pragma unroll
        for (int m = 0; m < 5; ++m) {
            const unsigned qd = m < 4 ? li + 32 * m : 128 + (li & 15);
            const unsigned off = (((row >> 4) * 18 + (qd >> 3)) * 64 + (qd & 3) * 16 + (row & 15)) * 16 + ((qd >> 2) & 1) * 8;
            const u32x2 o = {cvtpk(v[4 * m] * rstd, v[4 * m + 1] * rstd), cvtpk(v[4 * m + 2] * rstd, v[4 * m + 3] * rstd)};
            __builtin_amdgcn_raw_buffer_store_b64(o, rsX, off, 0, 0);
        }
    }
    SBAR();
    STAMP_AT(c);
    wait_vm<E::PL.waitA0>();
    kv_aread0(c, mkseq<16>{});
    SBAR();
    E::run(c);
    STAMP_AT(c);
    asm volatile("s_nop 7" ::: "memory"); SBAR();
    E::epi_all<3>(c);
    asm volatile("" :: "v"(c.pf));
    STAMP_AT(c);
    STAMP_AT(c);
    STAMP_OUT(c, ws, 0);
}

struct CfgGU {
    static constexpr int TT = 4, NKS = 18, NSTG = 15, AD = 3, ADX = 7, PRE_VM = 0, POST_PRO_VM = 0, NSLOT = 6, STB = 24576; static constexpr bool A_VIA_LDS = false;
    static constexpr int spass(int st) { return st / 3; }
    static constexpr int sk0(int st) { return 6 * (st % 3); }
    static constexpr int snks(int) { return 6; }
    static constexpr int nt(int p) { return p < 4 ? 4 : 2; }
    static constexpr int kseq(int st, int s) { return st * 6 + s; }
    static constexpr int nb(int st) { return st < 12 ? 2 : 1; }
    static constexpr int ppw(int) { return 6; }
    static constexpr int wissue(int g) { return g < 1 ? -1 : (g < 3 ? 0 : (g < 4 ? 1 : (g < 6 ? 2 : g - 3))); }
    static constexpr bool cert_next(int st) { return st > 0; }
    static constexpr bool bar_lgkm(int) { return false; }
    static constexpr int nepi(int) { return 4 * 27; }
    static constexpr int epi_cost(int, int e) { const int k = e % 27; return k < 24 ? (k % 3 == 2 ? 2 : 3) : 2; }
    static constexpr int epi_vm(int, int e) { return e % 27 == 26; }
    static constexpr int pro_list(int* o) { int n = 0;
        for (int i = 0; i < 6; ++i) o[n++] = OPC(OP_DMA, i);
        for (int k = 0; k < AD; ++k) for (int t = 0; t < 4; ++t) o[n++] = OPC(OP_ALD, k * 4 + t);
        return n; }
    struct Ctx {
        bf16x8 af[4][18]; f32x4 acc[2][4][4]; s16x4 w[2][4][2]; float ev[8], et[2]; u32x4 eo;
        lds_cptr rb[2], rbs, cb[2][2];
        unsigned mW, voffW, lane16; rsrc_t rsG, rsA, rsO; unsigned sW[5], sO[5], sA;
        STAMP_DECL
    };
    template <int A, int I> static DI void adma(Ctx&) {}
    template <int KS, int T> static DI void aread(Ctx&) {}
    template <int G, int I> static DI void dma(Ctx& c) { dmac<(G % NSLOT) * STB + I * 1024, (192 * (G % 3) + 16 * I) * (FF * 2)>(c.mW, c.voffW, c.rsG, c.sW[G / 3]); }
    template <int G, int S> static DI void readW(Ctx& c, int buf, int cc, int half) { c.w[buf][cc][half] = vtr(c.cb[G & 1][cc >> 1] + (cc & 1) * 12288 + S * 2048 + half * 1024); }
    template <int G, int I> static DI void setb(Ctx& c) { c.cb[G & 1][I] = (G >= 12 ? c.rbs : c.rb[I]) + (G % NSLOT) * STB; asm volatile("" : "+v"(c.cb[G & 1][I])); }
    template <int KS, int T> static DI void aload(Ctx& c) { ldfr<(KS & 3) * 1024, (KS < 16)>(c.af[T][KS], c.lane16, c.rsA, c.sA + (T * 18 + (KS & ~3)) * 1024); }
    template <int P, int KS, int CC, int T> static DI void mfma(Ctx& c, const bf16x8& wf) {
        if constexpr (KS < 16) mf_va<KS == 0>(c.acc[P & 1][CC][T], wf, c.af[T][KS]); else mf_vv<false>(c.acc[P & 1][CC][T], wf, c.af[T][KS]); }
    template <int P, int I> static DI void epi(Ctx& c) {
        constexpr int T = I / 27, k = I % 27;
        if constexpr (k < 24) {
            constexpr int V = k / 3, ph = k % 3, hf = V >> 2, e = V & 3;
            const float g = c.acc[P & 1][2 * hf][T][e], u = c.acc[P & 1][2 * hf + 1][T][e];
            if constexpr (ph == 0) { c.et[V & 1] = __builtin_amdgcn_exp2f(g * -1.4426950408889634f); asm volatile("" : "+v"(c.et[V & 1])); }
            else if constexpr (ph == 1) { c.ev[V] = g * u; c.et[V & 1] = __builtin_amdgcn_rcpf(1.0f + c.et[V & 1]); asm volatile("" : "+v"(c.et[V & 1]), "+v"(c.ev[V])); }
            else { c.ev[V] *= c.et[V & 1]; asm volatile("" : "+v"(c.ev[V])); }
        } else if constexpr (k < 26) { constexpr int hh = k - 24; c.eo[2 * hh] = cvtpk(c.ev[4 * hh], c.ev[4 * hh + 1]); c.eo[2 * hh + 1] = cvtpk(c.ev[4 * hh + 2], c.ev[4 * hh + 3]); asm volatile("" : "+v"(c.eo)); }
        else __builtin_amdgcn_raw_buffer_store_b128(c.eo, c.rsO, c.lane16, c.sO[P] + (unsigned)(T * 144 * 1024), STORE_AUX);
    }
};
DI float swiglu(float g, float u) { const float e = __builtin_amdgcn_exp2f(g * -1.4426950408889634f); return g * u * __builtin_amdgcn_rcpf(1.0f + e); }

__global__ __launch_bounds__(256, 1) void k_gu(const unsigned short* __restrict__ wg, const unsigned short* __restrict__ wu, char* __restrict__ ws) {
    using E = Eng<CfgGU>;
    extern __shared__ __attribute__((aligned(16))) char lds[];
    const unsigned tid = threadIdx.x, lane = tid & 63, g = lane >> 4, q = (lane & 15) >> 2, p = lane & 3;
    CfgGU::Ctx c;
    STAMP_AT(c);
    const unsigned wid = rfl(tid >> 6); c.mW = (unsigned)(uintptr_t)lds + wid * 6144; c.lane16 = lane * 16;
    const unsigned bid = blockIdx.x, xcd = bid & 7, idx = bid >> 3, rt = 4 * (xcd & 1) + (idx & 3), cg = 8 * (xcd >> 1) + (idx >> 2);
    const unsigned odd = cg & 1, f0 = cg * 144;
    c.rsG = mkrsrc(wid < 2 ? wg : wu, DM * FF * 2);
    c.rsA = mkrsrc(ws + OFF_HN, (unsigned)SZ_HN); c.rsO = mkrsrc(ws + OFF_ACT, (unsigned)SZ_ACT);
    const unsigned tt0 = rt * 16 + wid * 4;
#pragma unroll
    for (int pp = 0; pp < 5; ++pp) {
        const unsigned fa = pp < 4 ? f0 + odd * 16 + 32 * pp : (odd ? f0 - 16 : f0 + 128);
        c.sW[pp] = (wid & 1) * 96 * (FF * 2) + fa * 2;
        c.sO[pp] = (tt0 * 144 + fa / 32) * 1024;
    }
    c.voffW = (lane >> 2) * (FF * 2) + (((lane & 3) ^ (((lane >> 4) & 1) << 1)) << 4);
#pragma unroll
    for (int u = 0; u < 2; ++u) c.rb[u] = (lds_cptr)lds + (4 * g + q) * 64 + ((u ^ (g & 1)) << 5) + 8 * p;
    c.rbs = (lds_cptr)lds + (4 * g + q) * 64 + ((odd ^ (g & 1)) << 5) + 8 * p;
    c.sA = tt0 * 18 * 1024;
    E::prologue(c);
    E::run(c);
    STAMP_AT(c);
    asm volatile("s_nop 7" ::: "memory"); SBAR();
#pragma unroll
    for (int t = 0; t < 4; ++t) {
        float v[4];
#pragma unroll
        for (int e = 0; e < 4; ++e) v[e] = swiglu(c.acc[0][0][t][e], c.acc[0][1][t][e]);
        const u32x2 o = {cvtpk(v[0], v[1]), cvtpk(v[2], v[3])};
        __builtin_amdgcn_raw_buffer_store_b64(o, c.rsO, c.lane16 + odd * 8, c.sO[4] + (unsigned)(t * 144 * 1024), STORE_AUX);
    }
    STAMP_AT(c);
    STAMP_AT(c);
    STAMP_OUT(c, ws, 4);
}

template <int NKS_> struct CfgN {
    static constexpr int TT = 2, NKS = NKS_, N0 = NKS / 4, N1 = (NKS + 7) / 8, NSTG = N0 + N1, AD = 6, ADX = 14, PRE_VM = 0, POST_PRO_VM = 0, NSLOT = 6, STB = 24576; static constexpr bool A_VIA_LDS = false;
    static constexpr int spass(int st) { return st < N0 ? 0 : 1; }
    static constexpr int sk0(int st) { return st < N0 ? 4 * st : 8 * (st - N0); }
    static constexpr int snks(int st) { return st < N0 ? 4 : (NKS - 8 * (st - N0) < 8 ? NKS - 8 * (st - N0) : 8); }
    static constexpr int nt(int p) { return p == 0 ? 6 : 3; }
    static constexpr int kseq(int st, int s) { return st < N0 ? 4 * st + s : NKS + 8 * (st - N0) + s; }
    static constexpr int nb(int) { return 1; }
    static constexpr int ppw(int g) { return g < N0 ? 6 : snks(g) * 3 / 4; }
    static constexpr int wissue(int g) { return g < 1 ? -1 : (g < 3 ? 0 : (g < 5 ? 1 : g - 3)); }
    static constexpr bool cert_next(int st) { return st > 0; }
    static constexpr bool bar_lgkm(int) { return false; }
    static constexpr int nepi(int p) { return p == 0 ? 12 : 6; }
    static constexpr int epi_cost(int, int) { return 2; }
    static constexpr int epi_vm(int, int) { return 1; }
    static constexpr int pro_list(int* o) { int n = 0;
        for (int i = 0; i < 6; ++i) o[n++] = OPC(OP_DMA, i);
        for (int k = 0; k < AD; ++k) for (int t = 0; t < 2; ++t) o[n++] = OPC(OP_ALD, k * 4 + t);
        return n; }
    struct Ctx {
        bf16x8 af[2][NKS]; f32x4 acc0[6][2], acc1[3][2]; s16x4 w[2][6][2];
        lds_cptr rb, cb[2];
        unsigned mW, mWh, voffW[3], lane16; rsrc_t rsW, rsA, rsP; unsigned sW0, sW1, sW1h, sA, sP;
        STAMP_DECL
    };
    template <int A, int I> static DI void adma(Ctx&) {}
    template <int KS, int T> static DI void aread(Ctx&) {}
    template <int G, int I> static DI void dma(Ctx& c) {
        if constexpr (G < N0) dmac<(G % NSLOT) * STB + I * 1024, (128 * G + 32 * (I / 3)) * 1152>(c.mW, c.voffW[I % 3], c.rsW, c.sW0);
        else if constexpr (snks(G) == 8) dmac<(G % NSLOT) * STB + I * 1024, (256 * (G - N0) + 32 * (I / 3)) * 1152>(c.mW, c.voffW[I % 3], c.rsW, c.sW1);
        else dmac<(G % NSLOT) * STB + I * 1024, (256 * (G - N0)) * 1152>(c.mWh, c.voffW[I % 3], c.rsW, c.sW1h);
    }
    template <int G, int S> static DI void readW(Ctx& c, int buf, int cc, int half) { c.w[buf][cc][half] = vtr(c.cb[G & 1] + (cc / 3) * 12288 + S * 3072 + half * 1536 + (cc % 3) * 32); }
    template <int G, int I> static DI void setb(Ctx& c) { c.cb[G & 1] = c.rb + (G % NSLOT) * STB; asm volatile("" : "+v"(c.cb[G & 1])); }
    template <int KS, int T> static DI void aload(Ctx& c) { ldfr<(KS & 3) * 1024, (KS < 32)>(c.af[T][KS], c.lane16, c.rsA, c.sA + (T * NKS * 4 + (KS & ~3)) * 1024); }
    template <int P, int KS, int CC, int T> static DI void mfma(Ctx& c, const bf16x8& wf) {
        f32x4& a = P == 0 ? c.acc0[CC][T] : c.acc1[CC % 3][T];
        if constexpr (KS < 32) mf_va<KS == 0>(a, wf, c.af[T][KS]); else mf_vv<false>(a, wf, c.af[T][KS]); }
    template <int P, int I> static DI void epi(Ctx& c) {
        constexpr int cc = I >> 1, t = I & 1;
        __builtin_amdgcn_raw_buffer_store_b128(__builtin_bit_cast(u32x4, P == 0 ? c.acc0[cc][t] : c.acc1[cc % 3][t]), c.rsP, c.lane16, c.sP + (unsigned)(((t * 36 + 6 * P + cc) * 4) * 1024), STORE_AUX);
    }
    static DI void run(const unsigned short* W, const char* A, unsigned abytes, char* Pp, char* lds, char* ws, int kid) {
        using E = Eng<CfgN<NKS_>>;
        const unsigned tid = threadIdx.x, lane = tid & 63, g = lane >> 4, q = (lane & 15) >> 2, p = lane & 3;
        Ctx c;
        STAMP_AT(c);
        const unsigned wid = rfl(tid >> 6); c.mW = (unsigned)(uintptr_t)lds + wid * 6144; c.mWh = (unsigned)(uintptr_t)lds + wid * 3072; c.lane16 = lane * 16;
        const unsigned bid = blockIdx.x, xcd = bid & 7, idx = bid >> 3, sp = xcd >> 1, rt = 8 * (xcd & 1) + (idx & 7), cg = idx >> 3;
        c.rsW = mkrsrc(W, NKS * 128 * DM * 2); c.rsA = mkrsrc(A, abytes); c.rsP = mkrsrc(Pp, (unsigned)SZ_PART);
        const unsigned tt0 = rt * 8 + wid * 2, wb = sp * NKS * 32 * 1152 + cg * 288;
        c.sW0 = wb + (wid & 1) * 64 * 1152 + (wid >> 1) * 96;
        c.sW1 = wb + wid * 64 * 1152 + 192; c.sW1h = wb + wid * 32 * 1152 + 192;
        c.sA = (tt0 * NKS * 4 + sp * NKS) * 1024;
        c.sP = ((tt0 * 36 + cg * 9) * 4 + sp) * 1024;
#pragma unroll
        for (int b = 0; b < 3; ++b) { const unsigned ci = 64 * b + lane; c.voffW[b] = (ci / 6) * 1152 + (ci % 6) * 16; }
        c.rb = (lds_cptr)lds + (4 * g + q) * 96 + 8 * p;
        E::prologue(c);
        E::run(c);
        STAMP_AT(c);
        asm volatile("s_nop 7" ::: "memory"); SBAR();
        E::template epi_all<1>(c);
        STAMP_AT(c);
        STAMP_AT(c);
        STAMP_OUT(c, ws, kid);
    }
};
__global__ __launch_bounds__(256, 1) void k_o(const unsigned short* __restrict__ wo, char* __restrict__ ws) {
    extern __shared__ __attribute__((aligned(16))) char lds[];
    CfgN<32>::run(wo, ws + OFF_CTX, (unsigned)SZ_CTX, ws + OFF_P1, lds, ws, 2);
}
__global__ __launch_bounds__(256, 1) void k_d(const unsigned short* __restrict__ wd, char* __restrict__ ws) {
    extern __shared__ __attribute__((aligned(16))) char lds[];
    CfgN<36>::run(wd, ws + OFF_ACT, (unsigned)SZ_ACT, ws + OFF_P2, lds, ws, 5);
}
__global__ __launch_bounds__(256) void k_r1(const unsigned short* __restrict__ x, char* __restrict__ ws) {
    __shared__ float red[2][4][16];
    const unsigned tid = threadIdx.x, lane = tid & 63, wid = tid >> 6, r = lane & 15, g = lane >> 4, tt = blockIdx.x;
    const char* P = ws + OFF_P1 + ((size_t)(tt * 36 + wid * 9) * 4) * 1024 + lane * 16;
    f32x4 v[9]; float s = 0;
#pragma unroll
    for (int n = 0; n < 9; ++n) {
        const f32x4 a = *(const f32x4*)(P + (n * 4 + 0) * 1024), b = *(const f32x4*)(P + (n * 4 + 1) * 1024), c = *(const f32x4*)(P + (n * 4 + 2) * 1024), d = *(const f32x4*)(P + (n * 4 + 3) * 1024);
        const u32x2 xv = *(const u32x2*)(x + (size_t)(tt * 16 + r) * DM + (wid * 9 + n) * 16 + 4 * g);
        f32x4 hv = (a + b) + (c + d);
        hv[0] += __uint_as_float(xv[0] << 16); hv[1] += __uint_as_float(xv[0] & 0xffff0000u); hv[2] += __uint_as_float(xv[1] << 16); hv[3] += __uint_as_float(xv[1] & 0xffff0000u);
        const u32x2 hb = {cvtpk(hv[0], hv[1]), cvtpk(hv[2], hv[3])};
        *(u32x2*)(ws + OFF_H + ((size_t)(tt * 36 + wid * 9 + n) * 64 + lane) * 8) = hb;
        v[n] = f32x4{__uint_as_float(hb[0] << 16), __uint_as_float(hb[0] & 0xffff0000u), __uint_as_float(hb[1] << 16), __uint_as_float(hb[1] & 0xffff0000u)};
        s += (v[n][0] + v[n][1]) + (v[n][2] + v[n][3]);
    }
    s += __shfl_xor(s, 16); s += __shfl_xor(s, 32);
    if (lane < 16) red[0][wid][lane] = s;
    __syncthreads();
    const float mean = ((red[0][0][r] + red[0][1][r]) + (red[0][2][r] + red[0][3][r])) * (1.0f / DM);
    float qq = 0;
#pragma unroll
    for (int n = 0; n < 9; ++n) { v[n] -= mean; qq += (v[n][0] * v[n][0] + v[n][1] * v[n][1]) + (v[n][2] * v[n][2] + v[n][3] * v[n][3]); }
    qq += __shfl_xor(qq, 16); qq += __shfl_xor(qq, 32);
    if (lane < 16) red[1][wid][lane] = qq;
    __syncthreads();
    const float rstd = 1.0f / sqrtf(((red[1][0][r] + red[1][1][r]) + (red[1][2][r] + red[1][3][r])) * (1.0f / DM) + 1e-5f);
#pragma unroll
    for (int n = 0; n < 9; ++n) {
        const unsigned nt = wid * 9 + n;
        const u32x2 o = {cvtpk(v[n][0] * rstd, v[n][1] * rstd), cvtpk(v[n][2] * rstd, v[n][3] * rstd)};
        *(u32x2*)(ws + OFF_HN + ((size_t)(tt * 18 + (nt >> 1)) * 64 + lane) * 16 + (nt & 1) * 8) = o;
    }
}
__global__ __launch_bounds__(256) void k_r2(char* __restrict__ ws, unsigned short* __restrict__ out) {
    const unsigned tid = threadIdx.x, lane = tid & 63, wid = tid >> 6, r = lane & 15, g = lane >> 4, tt = blockIdx.x >> 1, hf = blockIdx.x & 1;
#pragma unroll
    for (int i = 0; i < 5; ++i) {
        const unsigned nl = wid + 4 * i; if (nl >= 18) break;
        const unsigned nt = hf * 18 + nl;
        const char* P = ws + OFF_P2 + ((size_t)(tt * 36 + nt) * 4) * 1024 + lane * 16;
        const f32x4 a = *(const f32x4*)(P), b = *(const f32x4*)(P + 1024), c = *(const f32x4*)(P + 2048), d = *(const f32x4*)(P + 3072);
        const u32x2 hb = *(const u32x2*)(ws + OFF_H + ((size_t)(tt * 36 + nt) * 64 + lane) * 8);
        f32x4 o = (a + b) + (c + d);
        o[0] += __uint_as_float(hb[0] << 16); o[1] += __uint_as_float(hb[0] & 0xffff0000u); o[2] += __uint_as_float(hb[1] << 16); o[3] += __uint_as_float(hb[1] & 0xffff0000u);
        *(u32x2*)(out + (size_t)(tt * 16 + r) * DM + nt * 16 + 4 * g) = u32x2{cvtpk(o[0], o[1]), cvtpk(o[2], o[3])};
    }
}

struct CfgQ {
    static constexpr int TT = 4, NKS = 18, NSTG = 6, AD = 3, ADX = 7, PRE_VM = 0, POST_PRO_VM = 0, STB = 24576, W0 = 65536; static constexpr bool A_VIA_LDS = false;
    static constexpr int spass(int st) { return st / 3; }
    static constexpr int sk0(int st) { return 6 * (st % 3); }
    static constexpr int snks(int) { return 6; }
    static constexpr int nt(int) { return 4; }
    static constexpr int kseq(int st, int s) { return st * 6 + s; }
    static constexpr int nb(int) { return 4; }
    static constexpr int ppw(int) { return 6; }
    static constexpr int wissue(int g) { return g < 1 ? -1 : (g < 3 ? 0 : g - 2 - (g == 5 ? 1 : 0) ); }
    static constexpr bool cert_next(int st) { return st > 0; }
    static constexpr bool bar_lgkm(int) { return false; }
    static constexpr int nepi(int) { return 16; }
    static constexpr int epi_cost(int, int) { return 3; }
    static constexpr int epi_vm(int, int) { return 1; }
    static constexpr int pro_list(int* o) { return CfgGU::pro_list(o); }
    struct Ctx {
        bf16x8 af[4][18]; f32x4 acc[2][4][4]; s16x4 w[2][4][2];
        lds_cptr rb[4], cb[2][4];
        unsigned mW, mK, voffW, lane16; rsrc_t rsW, rsA, rsK, rsV; unsigned sW, sA, sK, sV;
        STAMP_DECL
    };
    template <int A, int I> static DI void adma(Ctx&) {}
    template <int KS, int T> static DI void aread(Ctx&) {}
    template <int G, int I> static DI void dma(Ctx& c) { dmac<W0 + (G % 4) * STB + I * 1024, (192 * (G % 3) + 8 * I) * 8192 + (G / 3) * 128>(c.mW, c.voffW, c.rsW, c.sW); }
    template <int G, int S> static DI void readW(Ctx& c, int buf, int cc, int half) { c.w[buf][cc][half] = vtr(c.cb[G & 1][cc] + S * 4096 + half * 2048); }
    template <int G, int I> static DI void setb(Ctx& c) { c.cb[G & 1][I] = c.rb[I] + (G % 4) * STB; asm volatile("" : "+v"(c.cb[G & 1][I])); }
    template <int KS, int T> static DI void aload(Ctx& c) { ldfr<(KS & 3) * 1024, (KS < 16)>(c.af[T][KS], c.lane16, c.rsA, c.sA + (T * 18 + (KS & ~3)) * 1024); }
    template <int P, int KS, int CC, int T> static DI void mfma(Ctx& c, const bf16x8& wf) {
        if constexpr (KS < 16) mf_va<KS == 0>(c.acc[P][CC][T], wf, c.af[T][KS]); else mf_vv<false>(c.acc[P][CC][T], wf, c.af[T][KS]); }
    template <int P, int I> static DI void epi(Ctx& c) { dmac<(I >> 3) * 32768 + (I & 7) * 1024, (I & 7) * 1024>(c.mK, c.lane16, I < 8 ? c.rsK : c.rsV, I < 8 ? c.sK : c.sV); }
};
namespace k2 {
constexpr float QSCALE = 0.12751743082f;
struct Ctx {
    bf16x8 qf[4][4];
    f32x4 O[8][4], L[4];
    bf16x8 ones;
    bf16x8 fr[4];
    f32x4 S[4][4];
    u32x4 P[2][4];
    f32x4 NM[4];
    unsigned orv;
    float e[2][2];
    lds_cptr kb, vb, kbn, vbn;
    unsigned lds0, wid, lane16;
    rsrc_t rsK, rsV;
    unsigned sK, sV;
    STAMP_DECL
};
constexpr unsigned KSL[2] = {0u, 65536u};
DI unsigned kslot(int t) { return (t & 1) ? 65536u : 0u; }
DI unsigned vslot(int t3) { return t3 == 0 ? 32768u : (t3 == 1 ? 98304u : 131072u); }
DI void dma4(unsigned m0v, unsigned voff, rsrc_t rs, unsigned soff) {
    asm volatile("s_mov_b32 m0, %0\n\ts_nop 4\n\tbuffer_load_dwordx4 %1, %2, %3 offen lds\n\tbuffer_load_dwordx4 %1, %2, %3 offen offset:1024 lds\n\t"
                 "buffer_load_dwordx4 %1, %2, %3 offen offset:2048 lds\n\tbuffer_load_dwordx4 %1, %2, %3 offen offset:3072 lds" ::"s"(m0v), "v"(voff), "s"(rs), "s"(soff) : "m0", "memory");
}
template <int I> DI void dma1(unsigned m0v, unsigned voff, rsrc_t rs, unsigned soff) {
    asm volatile("s_mov_b32 m0, %0\n\ts_nop 0\n\tbuffer_load_dwordx4 %1, %2, %3 offen offset:%4 lds" ::"s"(m0v + (I >> 2) * 4096u), "v"(voff), "s"(rs), "s"(soff + (I >> 2) * 4096u), "i"((I & 3) * 1024) : "m0", "memory");
}
DI void dmaK(Ctx& c, int t) { const unsigned m = c.lds0 + kslot(t) + c.wid * 8192u, so = c.sK + (unsigned)t * 32768u; dma4(m, c.lane16, c.rsK, so); dma4(m + 4096u, c.lane16, c.rsK, so + 4096u); }
DI void dmaV(Ctx& c, int t, int t3) { const unsigned m = c.lds0 + vslot(t3) + c.wid * 8192u, so = c.sV + (unsigned)t * 32768u; dma4(m, c.lane16, c.rsV, so); dma4(m + 4096u, c.lane16, c.rsV, so + 4096u); }
DI bf16x8 rdf(lds_cptr b, int i) { return *(const LAS bf16x8*)(b + i * 1024); }
template <int MODE> DI void mf_qk(f32x4& s, const bf16x8& k, const bf16x8& q, const f32x4& nm) {
    if constexpr (MODE == 0) asm volatile("v_mfma_f32_16x16x32_bf16 %0, %1, %2, %0" : "+v"(s) : "v"(k), "a"(q));
    else if constexpr (MODE == 1) asm volatile("v_mfma_f32_16x16x32_bf16 %0, %1, %2, %3" : "=&v"(s) : "v"(k), "a"(q), "v"(nm));
    else asm volatile("v_mfma_f32_16x16x32_bf16 %0, %1, %2, 0" : "=&v"(s) : "v"(k), "a"(q));
}
DI void mf_pv(f32x4& o, const bf16x8& v, const u32x4& p) { asm volatile("v_mfma_f32_16x16x32_bf16 %0, %1, %2, %0" : "+a"(o) : "v"(v), "v"(p)); }
DI void mf_l(f32x4& o, const bf16x8& v, const u32x4& p) { asm volatile("v_mfma_f32_16x16x32_bf16 %0, %1, %2, %0" : "+a"(o) : "a"(v), "v"(p)); }
template <int U, int K> DI void sm_word(Ctx& c) {
    constexpr int TT = K >> 2, W = K & 3;
    const f32x4& s = c.S[2 * U + (W >> 1)][TT];
    const unsigned w = cvtpk(__builtin_amdgcn_exp2f(s[2 * (W & 1)]), __builtin_amdgcn_exp2f(s[2 * (W & 1) + 1]));
    c.P[U][TT][W] = w;
    if constexpr (W & 1) { c.orv = c.orv | c.P[U][TT][W - 1] | w; asm volatile("" : "+v"(c.orv)); }
    asm volatile("" : "+v"(c.P[U][TT]));
}
template <int U, int K, int H> DI void smE(Ctx& c) { constexpr int TT = K >> 2, W = K & 3; c.e[K & 1][H] = __builtin_amdgcn_exp2f(c.S[2 * U + (W >> 1)][TT][2 * (W & 1) + H]); asm volatile("" : "+v"(c.e[K & 1][H])); }
template <int U, int K> DI void smC(Ctx& c) { constexpr int TT = K >> 2, W = K & 3; c.P[U][TT][W] = cvtpk(c.e[K & 1][0], c.e[K & 1][1]); asm volatile("" : "+v"(c.P[U][TT])); }
template <int U, int K> DI void smR(Ctx& c) { constexpr int TT = K >> 2, W = K & 3; if constexpr (W & 1) { c.orv = c.orv | c.P[U][TT][W - 1] | c.P[U][TT][W]; asm volatile("" : "+v"(c.orv)); } }
template <int U, int K, int H> DI void smSlot(Ctx& c) {
    if constexpr (K < 16) smE<U, (K < 16 ? K : 0), H>(c);
    if constexpr (K >= 1) { if constexpr (H == 0) smC<U, (K >= 1 ? K - 1 : 0)>(c); else smR<U, (K >= 1 ? K - 1 : 0)>(c); }
}
template <int I> DI void dmaP(unsigned m0v, unsigned voff, rsrc_t rs, unsigned soff) {
    if constexpr ((I & 3) == 0) asm volatile("s_mov_b32 m0, %0\n\ts_nop 0\n\tbuffer_load_dwordx4 %1, %2, %3 offen lds" ::"s"(m0v + (I >> 2) * 4096u), "v"(voff), "s"(rs), "s"(soff + (I >> 2) * 4096u) : "m0", "memory");
    else asm volatile("buffer_load_dwordx4 %0, %1, %2 offen offset:%3 lds" ::"v"(voff), "s"(rs), "s"(soff + (I >> 2) * 4096u), "i"((I & 3) * 1024) : "m0", "memory");
}
template <int U, int MODE0, int NEXT, int SMU, int DM_, int M> DI void gapQ(Ctx& c, unsigned m0v, rsrc_t rs, unsigned so) {
    constexpr int f = M >> 2, tt = M & 3, a = 2 * U + (f >> 2), T = f & 3;
    mf_qk<T != 0 ? 0 : MODE0>(c.S[a][tt], c.fr[f & 3], c.qf[tt][T], c.NM[tt]);
    if constexpr (tt == 0) {
        if constexpr (f + 3 < 8) c.fr[(f + 3) & 3] = rdf(c.kb, 8 * U + f + 3);
        else if constexpr (NEXT == 0) c.fr[(f + 3) & 3] = rdf(c.vb, 8 * U + f + 3 - 8);
        else if constexpr (NEXT == 1) c.fr[(f + 3) & 3] = rdf(c.kb, 8 + f + 3 - 8);
    }
    if constexpr (SMU >= 0 && (tt == 1 || tt == 2)) smSlot<(SMU >= 0 ? SMU : 0), f, tt - 1>(c);
    if constexpr (DM_ == 1 && tt == 3) dmaP<f>(m0v, c.lane16, rs, so);
    SBAR();
}
template <int U, int NEXT, int SMU, int SMN, int DM_, int M> DI void gapP(Ctx& c, unsigned m0v, rsrc_t rs, unsigned so) {
    constexpr int f = M >> 2, tt = M & 3;
    if constexpr (f < 8) mf_pv(c.O[f][tt], c.fr[f & 3], c.P[U][tt]); else mf_l(c.L[tt], c.ones, c.P[U][tt]);
    if constexpr (tt == 0 && f < 8) {
        if constexpr (f + 3 < 8) c.fr[(f + 3) & 3] = rdf(c.vb, 8 * U + f + 3);
        else if constexpr (NEXT == 0) c.fr[(f + 3) & 3] = rdf(c.kb, 8 + f + 3 - 8);
        else if constexpr (NEXT == 1) c.fr[(f + 3) & 3] = rdf(c.kbn, f + 3 - 8);
        else if constexpr (NEXT == 2) c.fr[(f + 3) & 3] = rdf(c.vb, 8 + f + 3 - 8);
    }
    if constexpr (SMU >= 0 && SMN == 8 && (tt == 1 || tt == 2)) smSlot<(SMU >= 0 ? SMU : 0), 8 + f, tt - 1>(c);
    if constexpr (SMU >= 0 && SMN == 16 && (f < 8 || tt < 2)) smSlot<(SMU >= 0 ? SMU : 0), 2 * f + (tt >> 1), tt & 1>(c);
    if constexpr (DM_ == 1 && tt == 3 && f < 8) dmaP<(f & 7)>(m0v, c.lane16, rs, so);
    SBAR();
}
template <int U, int MODE0, int NEXT, int SMU, int DM_, int... Ms> DI void phQ_(Ctx& c, unsigned m0v, rsrc_t rs, unsigned so, iseq<Ms...>) { (gapQ<U, MODE0, NEXT, SMU, DM_, Ms>(c, m0v, rs, so), ...); }
template <int U, int NEXT, int SMU, int SMN, int DM_, int... Ms> DI void phP_(Ctx& c, unsigned m0v, rsrc_t rs, unsigned so, iseq<Ms...>) { (gapP<U, NEXT, SMU, SMN, DM_, Ms>(c, m0v, rs, so), ...); }
template <int U, int K0, int... Ks> DI void sm_words(Ctx& c, iseq<Ks...>) { (sm_word<U, K0 + Ks>(c), ...); }
DI float xmax(float m) { m = fmaxf(m, __shfl_xor(m, 16)); return fmaxf(m, __shfl_xor(m, 32)); }
template <int U> DI float rowmax(const Ctx& c, int tt) {
    const f32x4 &x = c.S[2 * U][tt], &y = c.S[2 * U + 1][tt];
    return xmax(fmaxf(fmaxf(fmaxf(x[0], x[1]), fmaxf(x[2], x[3])), fmaxf(fmaxf(y[0], y[1]), fmaxf(y[2], y[3]))));
}
template <int U> DI void mask(Ctx& c, int dq) {
    asm volatile("s_nop 7" : "+v"(dq) :: "memory"); SBAR();
#pragma unroll
    for (int a = 2 * U; a < 2 * U + 2; ++a)
#pragma unroll
        for (int tt = 0; tt < 4; ++tt)
#pragma unroll
            for (int e = 0; e < 4; ++e) if (16 * (a - tt) + e > dq) c.S[a][tt][e] = -__builtin_inff();
}
template <int U, bool HASO> DI void rescue(Ctx& c) {
    asm volatile("s_nop 7" ::: "memory"); SBAR();
#pragma unroll
    for (int tt = 0; tt < 4; ++tt) {
        const float dl = fmaxf(rowmax<U>(c, tt) + 7.0f, 0.0f), f = __builtin_amdgcn_exp2f(-dl);
#pragma unroll
        for (int a = 0; a < 4; ++a) if (HASO || (a >> 1) == U) c.S[a][tt] -= dl;
        c.NM[tt] -= dl;
#pragma unroll
        for (int dt = 0; dt < 8; ++dt) { acc_scale(c.O[dt][tt], f); SBAR(); }
        acc_scale(c.L[tt], f); SBAR();
    }
    sm_words<U, 0>(c, mkseq<16>{});
    asm volatile("s_nop 1" ::: "memory");
}
template <int U, bool HASO> DI void check(Ctx& c) {
    if (__builtin_expect(__builtin_amdgcn_ballot_w64((c.orv & 0x40004000u) != 0u) != 0ull, 0)) rescue<U, HASO>(c);
    c.orv = 0;
}
DI void block(Ctx& c, const int NS, const int dq, const lds_cptr fb, const unsigned recK, const unsigned recV, const char* ws) {
#pragma unroll
    for (int tt = 0; tt < 4; ++tt) {
#pragma unroll
        for (int dt = 0; dt < 8; ++dt) { c.O[dt][tt] = f32x4{0.f, 0.f, 0.f, 0.f}; asm volatile("" : "+a"(c.O[dt][tt])); }
        c.L[tt] = f32x4{0.f, 0.f, 0.f, 0.f}; asm volatile("" : "+a"(c.L[tt]));
    }
    c.orv = 0;
    c.kb = fb; asm volatile("" : "+v"(c.kb));
    wait_vm_bar<0>();
    SBAR();
#pragma unroll
    for (int f = 0; f < 3; ++f) c.fr[f] = rdf(c.kb, f);
    SBAR();
    phQ_<0, 2, 1, -1, 0>(c, 0u, c.rsV, 0u, mkseq<32>{});
    if (NS == 1) mask<0>(c, dq); else asm volatile("s_nop 7" ::: "memory");
    SBAR();
#pragma unroll
    for (int tt = 0; tt < 4; ++tt) {
        const float m = fmaxf(rowmax<0>(c, tt), -1e30f) + 7.0f;
        c.S[0][tt] -= m; c.S[1][tt] -= m;
        c.NM[tt] = f32x4{-m, -m, -m, -m};
    }
    asm volatile("s_nop 1" : "+v"(c.NM[0]), "+v"(c.NM[1]), "+v"(c.NM[2]), "+v"(c.NM[3]));
    SBAR();
    phQ_<1, 1, 2, 0, 0>(c, 0u, c.rsV, 0u, mkseq<32>{});
    smC<0, 7>(c);
    sm_words<0, 8>(c, mkseq<8>{});
    if (NS == 1) mask<1>(c, dq);
    c.orv = 0;
    barrier_lds();
    if (NS > 2) dmaK(c, 2);
    c.kb = fb + 65536; c.vb = fb + 32768; asm volatile("" : "+v"(c.kb), "+v"(c.vb));
    if (NS > 1) {
#pragma unroll
        for (int f = 0; f < 3; ++f) c.fr[f] = rdf(c.kb, f);
    } else {
#pragma unroll
        for (int f = 0; f < 3; ++f) c.fr[f] = rdf(c.vb, f);
    }
    SBAR();
    int t3 = 0;
    for (int t = 0; t + 1 < NS; ++t) {
        const int t3n = t3 == 2 ? 0 : t3 + 1;
        const bool last = t + 2 == NS;
        phQ_<0, 1, 0, 1, 1>(c, c.lds0 + vslot(t3n) + c.wid * 8192u, c.rsV, c.sV + (unsigned)(t + 1) * 32768u, mkseq<32>{});
        if (last) mask<0>(c, dq);
        phP_<0, 0, 1, 8, 0>(c, 0u, c.rsV, 0u, mkseq<36>{});
        check<1, true>(c);
        c.kbn = last ? fb + vslot(t3n) : fb + kslot(t); asm volatile("" : "+v"(c.kbn));
        phQ_<1, 1, 0, 0, 0>(c, 0u, c.rsV, 0u, mkseq<32>{});
        if (last) mask<1>(c, dq);
        wait_vm_bar<0>();
        SBAR();
        const rsrc_t rsKd = mkrsrc(ws + OFF_KF, t + 3 < NS ? recK : 0u);
        phP_<1, 1, 0, 8, 1>(c, c.lds0 + kslot(t + 1) + c.wid * 8192u, rsKd, c.sK + (unsigned)(t + 3) * 32768u, mkseq<36>{});
        check<0, true>(c);
        c.kb = c.kbn; c.vb = fb + vslot(t3n); asm volatile("" : "+v"(c.vb));
        t3 = t3n;
    }
    phP_<0, 2, 1, 16, 0>(c, 0u, c.rsV, 0u, mkseq<36>{});
    check<1, false>(c);
    phP_<1, 3, -1, 0, 0>(c, 0u, c.rsV, 0u, mkseq<36>{});
}
DI void merge_store(Ctx& c, char* lds, unsigned lane, rsrc_t rsC, unsigned sC) {
    const unsigned kh = c.wid >> 1;
    asm volatile("" : "+v"(lane));
    LAS f32x2* mlw = (LAS f32x2*)(lds_cptr)(lds + 65536) + c.wid * 256 + lane; LAS f32x2* mlr = (LAS f32x2*)(lds_cptr)(lds + 65536) + (c.wid ^ 2) * 256 + lane;
    asm volatile("" : "+v"(mlw), "+v"(mlr));
    float m[4], l[4], w[4];
#pragma unroll
    for (int tt = 0; tt < 4; ++tt) { m[tt] = -c.NM[tt][0]; l[tt] = acc_rd(c.L[tt][0]); mlw[tt * 64] = f32x2{m[tt], l[tt]}; }
    barrier_lds();
#pragma unroll
    for (int tt = 0; tt < 4; ++tt) {
        const f32x2 o = mlr[tt * 64];
        const float M = fmaxf(m[tt], o[0]), f = __builtin_amdgcn_exp2f(m[tt] - M), f2 = __builtin_amdgcn_exp2f(o[0] - M);
        w[tt] = f * __builtin_amdgcn_rcpf(f * l[tt] + f2 * o[1]);
    }
    LAS f32x4* ob = (LAS f32x4*)(lds_cptr)(lds + 98304);
    LAS f32x4* ow = ob + c.wid * 1024 + lane; LAS f32x4* orr = ob + (c.wid ^ 2) * 1024 + lane;
    asm volatile("" : "+v"(ow), "+v"(orr));
    if (kh == 0) {
#pragma unroll
        for (int d = 0; d < 4; ++d)
#pragma unroll
            for (int tt = 0; tt < 4; ++tt) { ow[(d * 4 + tt) * 64] = acc_rd4(c.O[4 + d][tt]) * w[tt]; if (tt & 1) SBAR(); }
        barrier_lds();
#pragma unroll
        for (int T = 0; T < 2; ++T)
#pragma unroll
            for (int tt = 0; tt < 4; ++tt) {
                const f32x4 x = orr[((2 * T) * 4 + tt) * 64] + acc_rd4(c.O[2 * T][tt]) * w[tt], y = orr[((2 * T + 1) * 4 + tt) * 64] + acc_rd4(c.O[2 * T + 1][tt]) * w[tt];
                const u32x4 o = {cvtpk(x[0], x[1]), cvtpk(x[2], x[3]), cvtpk(y[0], y[1]), cvtpk(y[2], y[3])};
                __builtin_amdgcn_raw_buffer_store_b128(o, rsC, c.lane16, sC + (unsigned)((tt * 128 + T) * 1024), 0);
            }
    } else {
#pragma unroll
        for (int d = 0; d < 4; ++d)
#pragma unroll
            for (int tt = 0; tt < 4; ++tt) { ow[(d * 4 + tt) * 64] = acc_rd4(c.O[d][tt]) * w[tt]; if (tt & 1) SBAR(); }
        barrier_lds();
#pragma unroll
        for (int T = 0; T < 2; ++T)
#pragma unroll
            for (int tt = 0; tt < 4; ++tt) {
                const f32x4 x = orr[((2 * T) * 4 + tt) * 64] + acc_rd4(c.O[4 + 2 * T][tt]) * w[tt], y = orr[((2 * T + 1) * 4 + tt) * 64] + acc_rd4(c.O[5 + 2 * T][tt]) * w[tt];
                const u32x4 o = {cvtpk(x[0], x[1]), cvtpk(x[2], x[3]), cvtpk(y[0], y[1]), cvtpk(y[2], y[3])};
                __builtin_amdgcn_raw_buffer_store_b128(o, rsC, c.lane16, sC + (unsigned)((tt * 128 + 2 + T) * 1024), 0);
            }
    }
}
}

__global__ __launch_bounds__(256, 1) void k_attn(const unsigned short* __restrict__ wq, char* __restrict__ ws) {
    using namespace k2;
    extern __shared__ __attribute__((aligned(16))) char lds[];
    const unsigned tid = threadIdx.x, lane = tid & 63, r = lane & 15, g = lane >> 4, q = (lane & 15) >> 2, p = lane & 3;
    const unsigned wid = rfl(tid >> 6), rg = wid & 1, kh = wid >> 1;
    const unsigned bid = blockIdx.x, xcd = bid & 7, idx = bid >> 3, h = 4 * xcd + (idx >> 3), pi = idx & 7, blkA = 15 - pi, blkB = pi;
    Ctx c;
    STAMP_AT(c);
    c.wid = wid; c.lds0 = (unsigned)(uintptr_t)lds; c.lane16 = lane * 16;
    c.rsK = mkrsrc(ws + OFF_KF, (unsigned)SZ_KF); c.rsV = mkrsrc(ws + OFF_VF, (unsigned)SZ_VF);
    c.sK = h * 128 * 4096 + wid * 8192; c.sV = h * 64 * 8192 + wid * 8192;
    const rsrc_t rsC = mkrsrc(ws + OFF_CTX, (unsigned)SZ_CTX);
    const rsrc_t rsQ = mkrsrc(ws + OFF_QS, (unsigned)SZ_QS);
    {
        CfgQ::Ctx qc;
        qc.mW = c.lds0 + wid * 6144; qc.mK = c.lds0 + wid * 8192; qc.lane16 = c.lane16;
        qc.rsW = mkrsrc(wq, DM * 4096 * 2); qc.rsK = c.rsK; qc.rsV = c.rsV; qc.sK = c.sK; qc.sV = c.sV;
        qc.sW = wid * 48 * 8192 + h * 256;
        { const unsigned r8 = lane >> 3; qc.voffW = r8 * 8192 + (((lane & 7) ^ (((r8 >> 1) & 3) << 1)) << 4); }
        { const unsigned row = 4 * g + q, f = (row >> 1) & 3;
#pragma unroll
          for (int cc = 0; cc < 4; ++cc) qc.rb[cc] = (lds_cptr)lds + CfgQ::W0 + row * 128 + ((cc ^ f) << 5) + 8 * p; }
        qc.rsA = mkrsrc(ws + OFF_XN, (unsigned)SZ_XN);
        qc.sA = (((kh ? blkB : blkA) * 8 + rg * 4) * 18) * 1024;
        Eng<CfgQ>::prologue(qc);
        Eng<CfgQ>::run(qc);
        asm volatile("s_nop 7" ::: "memory");
        barrier_lds();
        dmaK(c, 1);
        LAS u32x4* qx = (LAS u32x4*)((lds_cptr)lds + 98304) + lane;
        u32x4 qo[16];
#pragma unroll
        for (int tt = 0; tt < 4; ++tt)
#pragma unroll
            for (int T = 0; T < 4; ++T) {
                const f32x4 x = qc.acc[T >> 1][2 * (T & 1)][tt] * QSCALE, y = qc.acc[T >> 1][2 * (T & 1) + 1][tt] * QSCALE;
                qo[tt * 4 + T] = u32x4{cvtpk(x[0], x[1]), cvtpk(x[2], x[3]), cvtpk(y[0], y[1]), cvtpk(y[2], y[3])};
            }
        if (kh == 0) {
#pragma unroll
            for (int i = 0; i < 16; ++i) qx[(rg * 16 + i) * 64] = qo[i];
        } else {
#pragma unroll
            for (int i = 0; i < 16; ++i) __builtin_amdgcn_raw_buffer_store_b128(qo[i], rsQ, c.lane16, (bid * 2 + rg) * 16384 + i * 1024, 0);
        }
        barrier_lds();
#pragma unroll
        for (int tt = 0; tt < 4; ++tt)
#pragma unroll
            for (int T = 0; T < 4; ++T) c.qf[tt][T] = __builtin_bit_cast(bf16x8, qx[(rg * 16 + tt * 4 + T) * 64]);
#pragma unroll
        for (int tt = 0; tt < 4; ++tt)
#pragma unroll
            for (int T = 0; T < 4; ++T) asm volatile("" : "+a"(c.qf[tt][T]));
    }
    STAMP_AT(c);
    const lds_cptr fb = (lds_cptr)lds + kh * 16384 + lane * 16;
    c.ones = bf16x8{0x3f80, 0x3f80, 0x3f80, 0x3f80, 0x3f80, 0x3f80, 0x3f80, 0x3f80}; asm volatile("" : "+a"(c.ones));
    const int dq = (int)(64 * rg + r) - (int)(64 * kh + 4 * g);
    for (int pass = 0; pass < 2; ++pass) {
        const unsigned blk = pass ? blkB : blkA;
        block(c, (int)blk + 1, dq, fb, (unsigned)SZ_KF, (unsigned)SZ_VF, ws);
        STAMP_AT(c);
        asm volatile("s_nop 7" ::: "memory");
        wait_vm<0>();
        barrier_lds();
        if (pass == 0) {
            dmaK(c, 0); dmaV(c, 0, 0);
            unsigned qo_ = c.lane16 + (bid * 2 + rg) * 16384; asm volatile("" : "+v"(qo_));
#pragma unroll
            for (int tt = 0; tt < 4; ++tt)
                asm volatile("s_nop 4\n\tbuffer_load_dwordx4 %0, %4, %5, 0 offen\n\tbuffer_load_dwordx4 %1, %4, %5, 0 offen offset:1024\n\tbuffer_load_dwordx4 %2, %4, %5, 0 offen offset:2048\n\tbuffer_load_dwordx4 %3, %4, %5, 0 offen offset:3072"
                             : "=&a"(c.qf[tt][0]), "=&a"(c.qf[tt][1]), "=&a"(c.qf[tt][2]), "=&a"(c.qf[tt][3]) : "v"(qo_ + tt * 4096), "s"(rsQ) : "memory");
        }
        merge_store(c, lds, lane, rsC, ((blk * 8 + rg * 4) * 128 + h * 4) * 1024);
        if (pass == 0) { barrier_lds(); dmaK(c, 1); }
        STAMP_AT(c);
    }
    STAMP_AT(c);
    STAMP_OUT(c, ws, 1);
}
extern "C" long long ws_offset(int i) {
    const size_t o[] = {OFF_XN, OFF_KF, OFF_VF, OFF_CTX, OFF_HN, OFF_ACT};
    return (long long)o[i];
}

extern "C" void kernel_launch(void* const* d_in, const int* in_sizes, int n_in, void* d_out, int out_size, void* d_ws, size_t ws_size, hipStream_t stream) {
    const unsigned short* x = (const unsigned short*)d_in[0];
    const unsigned short* ckv = (const unsigned short*)d_in[1];
    const unsigned short* wq = (const unsigned short*)d_in[2];
    const unsigned short* wuk = (const unsigned short*)d_in[3];
    const unsigned short* wuv = (const unsigned short*)d_in[4];
    const unsigned short* wo = (const unsigned short*)d_in[5];
    const unsigned short* wg = (const unsigned short*)d_in[7];
    const unsigned short* wu = (const unsigned short*)d_in[8];
    const unsigned short* wd = (const unsigned short*)d_in[9];
    char* ws = (char*)d_ws;
    (void)hipFuncSetAttribute((const void*)k_kv, hipFuncAttributeMaxDynamicSharedMemorySize, 163840);
    (void)hipFuncSetAttribute((const void*)k_attn, hipFuncAttributeMaxDynamicSharedMemorySize, 163840);
    (void)hipFuncSetAttribute((const void*)k_o, hipFuncAttributeMaxDynamicSharedMemorySize, 163840);
    (void)hipFuncSetAttribute((const void*)k_gu, hipFuncAttributeMaxDynamicSharedMemorySize, 163840);
    (void)hipFuncSetAttribute((const void*)k_d, hipFuncAttributeMaxDynamicSharedMemorySize, 163840);
    k_kv<<<256, 256, 163840, stream>>>(x, ckv, wuk, wuv, ws, wq, wo, wg, wu, wd);
    k_attn<<<256, 256, 163840, stream>>>(wq, ws);
    k_o<<<256, 256, 147456, stream>>>(wo, ws);
    k_r1<<<128, 256, 0, stream>>>(x, ws);
    k_gu<<<256, 256, 147456, stream>>>(wg, wu, ws);
    k_d<<<256, 256, 147456, stream>>>(wd, ws);
    k_r2<<<256, 256, 0, stream>>>(ws, (unsigned short*)d_out);
}
```

```cpp
#include <hip/hip_runtime.h>
#include <stdint.h>
#include <utility>

#define LAS __attribute__((address_space(3)))
#define DI __device__ __forceinline__
typedef unsigned u32x4 __attribute__((ext_vector_type(4)));
typedef unsigned u32x2 __attribute__((ext_vector_type(2)));
typedef short bf16x8 __attribute__((ext_vector_type(8)));
typedef short s16x4 __attribute__((ext_vector_type(4)));
typedef float f32x4 __attribute__((ext_vector_type(4)));
typedef float f32x2 __attribute__((ext_vector_type(2)));
typedef __bf16 bf16x2_t __attribute__((ext_vector_type(2)));
typedef LAS const char* lds_cptr;
typedef __amdgpu_buffer_rsrc_t rsrc_t;
template <int... Is> using iseq = std::integer_sequence<int, Is...>;
template <int N> using mkseq = std::make_integer_sequence<int, N>;
#define SBAR() __builtin_amdgcn_sched_barrier(0)

constexpr int S_ = 2048, DM = 576, DC = 512, FF = 4608;
constexpr size_t SZ_CNT = 262656, SZ_XN = (size_t)128 * 18 * 1024, SZ_KF = (size_t)32 * 128 * 4 * 1024, SZ_VF = SZ_KF, SZ_CTX = (size_t)128 * 128 * 1024, SZ_HN = SZ_XN, SZ_ACT = (size_t)128 * 144 * 1024;
constexpr size_t SZ_PART = (size_t)64 * 36 * 4 * 1024, SZ_H = (size_t)256 * 256 * 40, SZ_QS = (size_t)128 * 4 * 16384, SZ_PO = (size_t)128 * 4 * 18432;
constexpr size_t OFF_CNT = 0, OFF_XN = SZ_CNT, OFF_KF = OFF_XN + SZ_XN, OFF_VF = OFF_KF + SZ_KF, OFF_CTX = OFF_VF + SZ_VF, OFF_HN = OFF_CTX + SZ_CTX, OFF_ACT = OFF_HN + SZ_HN;
constexpr size_t OFF_P1 = OFF_ACT + SZ_ACT, OFF_P2 = OFF_P1 + 2 * SZ_PART, OFF_H = OFF_P2 + SZ_PART, OFF_QS = OFF_H + SZ_H, OFF_PO = OFF_QS + SZ_QS, OFF_END = OFF_PO + SZ_PO;
constexpr int CNT_A = 0, CNT_O = 128, CNT_H = 144, CNT_G = 152, CNT_D = 184, CNT_X = 200, CNT_K = 208, CNT_Q = 256, CNT_P = 384, CNT_N = 512;
constexpr int SC1 = 16;
constexpr float QSCALE = 0.12751743082f;

struct Stamp {};
#define STAMP_AT(s)
#define STAMP_OUT(s, ws, kid)
DI rsrc_t mkrsrc(const void* p, unsigned bytes) { return __builtin_amdgcn_make_buffer_rsrc((void*)p, (short)0, (int)bytes, 0x00020000); }
DI unsigned rfl(unsigned x) { return __builtin_amdgcn_readfirstlane(x); }
DI unsigned fresh_lane() { unsigned l; asm volatile("v_mbcnt_lo_u32_b32 %0, -1, 0\n\tv_mbcnt_hi_u32_b32 %0, -1, %0" : "=v"(l)); return l; }
DI s16x4 vtr(lds_cptr p) { return __builtin_amdgcn_ds_read_tr16_b64_v4i16((LAS s16x4*)p); }
DI bf16x8 cat(s16x4 a, s16x4 b) { return __builtin_shufflevector(a, b, 0, 1, 2, 3, 4, 5, 6, 7); }
DI unsigned cvtpk(float lo, float hi) { f32x2 v = {lo, hi}; return __builtin_bit_cast(unsigned, __builtin_convertvector(v, bf16x2_t)); }
DI float bflo(unsigned w) { return __uint_as_float(w << 16); }
DI float bfhi(unsigned w) { return __uint_as_float(w & 0xffff0000u); }
template <int N> DI void wait_vm() { asm volatile("s_waitcnt vmcnt(%0)" ::"i"(N > 63 ? 63 : N) : "memory"); }
template <int N> DI void wait_vm_bar() { asm volatile("s_waitcnt vmcnt(%0)\n\ts_barrier" ::"i"(N > 63 ? 63 : N) : "memory"); }
DI void wait_lgkm0() { asm volatile("s_waitcnt lgkmcnt(0)" ::: "memory"); }
template <int N> DI void wait_lgkm_known() { __builtin_amdgcn_s_waitcnt(0xC07F | (N << 8)); }
DI void barrier_lds() { asm volatile("s_waitcnt lgkmcnt(0)\n\ts_barrier" ::: "memory"); }
DI void barrier_all() { asm volatile("s_waitcnt vmcnt(0) lgkmcnt(0)\n\ts_barrier" ::: "memory"); }
template <bool Z> DI void mf_va(f32x4& c, const bf16x8& w, const bf16x8& a) {
    if constexpr (Z) asm volatile("v_mfma_f32_16x16x32_bf16 %0, %1, %2, 0" : "=&v"(c) : "v"(w), "a"(a));
    else asm volatile("v_mfma_f32_16x16x32_bf16 %0, %1, %2, %0" : "+v"(c) : "v"(w), "a"(a));
}
template <bool Z> DI void mf_av(f32x4& c, const bf16x8& a, const bf16x8& w) {
    if constexpr (Z) asm volatile("v_mfma_f32_16x16x32_bf16 %0, %1, %2, 0" : "=&v"(c) : "a"(a), "v"(w));
    else asm volatile("v_mfma_f32_16x16x32_bf16 %0, %1, %2, %0" : "+v"(c) : "a"(a), "v"(w));
}
template <bool Z> DI void mf_vv(f32x4& c, const bf16x8& w, const bf16x8& a) {
    if constexpr (Z) asm volatile("v_mfma_f32_16x16x32_bf16 %0, %1, %2, 0" : "=&v"(c) : "v"(w), "v"(a));
    else asm volatile("v_mfma_f32_16x16x32_bf16 %0, %1, %2, %0" : "+v"(c) : "v"(w), "v"(a));
}
DI float acc_rd(float a) { float t; asm volatile("v_accvgpr_read_b32 %0, %1" : "=v"(t) : "a"(a)); return t; }
DI float acc_wr(float v) { float a; asm volatile("v_accvgpr_write_b32 %0, %1" : "=a"(a) : "v"(v)); return a; }
DI f32x4 acc_rd4(const f32x4& a) { return f32x4{acc_rd(a[0]), acc_rd(a[1]), acc_rd(a[2]), acc_rd(a[3])}; }
DI void acc_scale(f32x4& a, float f) { const f32x4 t = acc_rd4(a); a = f32x4{acc_wr(t[0] * f), acc_wr(t[1] * f), acc_wr(t[2] * f), acc_wr(t[3] * f)}; asm volatile("" : "+a"(a)); }
template <int CTRL> DI float dpp(float v) { return __builtin_bit_cast(float, __builtin_amdgcn_update_dpp(0, __builtin_bit_cast(int, v), CTRL, 0xf, 0xf, true)); }
DI float swap16_add(float s) { auto r = __builtin_amdgcn_permlane16_swap(__float_as_uint(s), __float_as_uint(s), false, false); return __uint_as_float(r[0]) + __uint_as_float(r[1]); }
DI float swap32_add(float s) { auto r = __builtin_amdgcn_permlane32_swap(__float_as_uint(s), __float_as_uint(s), false, false); return __uint_as_float(r[0]) + __uint_as_float(r[1]); }
DI float swap16_max(float s) { auto r = __builtin_amdgcn_permlane16_swap(__float_as_uint(s), __float_as_uint(s), false, false); return fmaxf(__uint_as_float(r[0]), __uint_as_float(r[1])); }
DI float swap32_max(float s) { auto r = __builtin_amdgcn_permlane32_swap(__float_as_uint(s), __float_as_uint(s), false, false); return fmaxf(__uint_as_float(r[0]), __uint_as_float(r[1])); }
DI float sum32(float s) {
    s += dpp<0xB1>(s); s += dpp<0x4E>(s); s += dpp<0x141>(s); s += dpp<0x140>(s);
    return swap16_add(s);
}
template <int MO, int SO> DI void dma_s(unsigned& so, unsigned mb, unsigned sb) {
    asm volatile("s_add_u32 m0, %1, %3\n\ts_add_u32 %0, %2, %4" : "=&s"(so) : "s"(mb), "s"(sb), "i"(MO), "i"(SO) : "m0", "scc", "memory");
}
DI void dma_l(unsigned voff, rsrc_t rs, unsigned so) { asm volatile("buffer_load_dwordx4 %0, %1, %2 offen lds" ::"v"(voff), "s"(rs), "s"(so) : "m0", "memory"); }
template <int IMM, bool AG, bool COH> DI void ldfr(bf16x8& d, unsigned voff, rsrc_t rs, unsigned soff) {
    if constexpr (AG && COH) asm volatile("buffer_load_dwordx4 %0, %1, %2, %3 offen offset:%4 sc1" : "=a"(d) : "v"(voff), "s"(rs), "s"(soff), "i"(IMM) : "memory");
    else if constexpr (AG) asm volatile("buffer_load_dwordx4 %0, %1, %2, %3 offen offset:%4" : "=a"(d) : "v"(voff), "s"(rs), "s"(soff), "i"(IMM) : "memory");
    else if constexpr (COH) asm volatile("buffer_load_dwordx4 %0, %1, %2, %3 offen offset:%4 sc1" : "=v"(d) : "v"(voff), "s"(rs), "s"(soff), "i"(IMM) : "memory");
    else asm volatile("buffer_load_dwordx4 %0, %1, %2, %3 offen offset:%4" : "=v"(d) : "v"(voff), "s"(rs), "s"(soff), "i"(IMM) : "memory");
}
typedef __attribute__((address_space(1))) unsigned long long gu64;
constexpr size_t OFF_EPOCH = (size_t)CNT_N * 512;
struct Sync { char* ws; unsigned wid; unsigned long long tag; };
DI void signal(const Sync& y, unsigned grp, unsigned slot) {
    if (y.wid == 0) { if (fresh_lane() == 0) __hip_atomic_store((gu64*)(y.ws + OFF_CNT) + grp * 64 + slot, y.tag, __ATOMIC_RELAXED, __HIP_MEMORY_SCOPE_AGENT); }
}
DI void poll(const Sync& y, unsigned grp, unsigned n) {
    if (y.wid == 0) {
        const unsigned l = fresh_lane();
        const gu64* p = (gu64*)(y.ws + OFF_CNT) + grp * 64 + (l < n ? l : 0);
        const unsigned long long t0 = __builtin_amdgcn_s_memrealtime();
        while (__builtin_amdgcn_ballot_w64(__hip_atomic_load(p, __ATOMIC_RELAXED, __HIP_MEMORY_SCOPE_AGENT) != y.tag) != 0ull) {
            if (__builtin_amdgcn_s_memrealtime() - t0 > 20000000ull) break;
            __builtin_amdgcn_s_sleep(1);
        }
        __builtin_amdgcn_s_waitcnt(0xC07F);
    }
}
DI void poll_issue(const Sync& y, unsigned grp, unsigned voff8, unsigned long long& v) {
    asm volatile("global_load_dwordx2 %0, %1, %2 sc1" : "=v"(v) : "v"(voff8), "s"(y.ws + OFF_CNT + grp * 512) : "memory"); }
DI void poll_check(const Sync& y, unsigned grp, unsigned n, unsigned long long v) { if (__builtin_expect(__builtin_amdgcn_ballot_w64(v != y.tag) != 0ull, 0)) { Sync z = y; z.wid = 0; poll(z, grp, n); } }
DI void await(const Sync& y, unsigned grp, unsigned n) { poll(y, grp, n); asm volatile("s_barrier" ::: "memory"); }
#define KV_AUX 16

enum : int { OP_RD = 1, OP_DMS, OP_DML, OP_ALD, OP_EPI, OP_SETB, OP_AMS, OP_AML, OP_ARD, OP_AWAIT, OP_PL };
constexpr int OPC(int k, int a) { return k * 65536 + a; }
template <class C> struct Eng {
    using Ctx = typename C::Ctx;
    static constexpr int gaps(int st) { return C::snks(st) * C::nt(C::spass(st)) * C::TT; }
    static constexpr int gstart(int st) { int g = 0; for (int i = 0; i < st; ++i) g += gaps(i); return g; }
    static constexpr int NG = gstart(C::NSTG);
    struct Plan { int nop[NG]; int op[NG][6]; int warg[NG]; int lw[NG]; int waitA[C::NKS * C::APASS + 1]; int waitW[C::NSTG]; int waitP[C::NSTG]; int left; int waitA0, waitPre, waitPre1; };
    static constexpr Plan mk() {
        Plan p{}; int cnt = C::PRE_VM;
        int posA[C::NKS * C::APASS + 8] = {}, posE = 0, posP = 0, posW[C::NSTG] = {}, numA[8] = {}, numW[C::NSTG] = {};
        if (C::A_VIA_LDS) { cnt += 8; posA[0] = cnt; }
        for (int g = 0; g < C::NSTG; ++g) if (C::wissue(g) < 0) { cnt += C::ppw(g); posW[g] = cnt; numW[g] = C::ppw(g); }
        if (C::A_VIA_LDS) { cnt += 8; posA[1] = cnt; numA[1] = 8; }
        for (int k = 0; k < C::AD; ++k) { cnt += C::TT; posA[k] = cnt; }
        p.waitPre = cnt - C::PRE_VM;
        cnt += C::POST_PRO_VM;
        p.waitA0 = cnt - posA[0];
        int qh[200] = {}, qhn = 0, qhh = 0;
        int qm[400] = {}, qmn = 0, qmh = 0;
        int ql[900] = {}, qln = 0, qlh = 0;
        int G = 0, nexta = C::AD, epi_num = 0, epi_den = 1, epi_g0 = 0, epi_done = 0; bool prev_plain = false;
        for (int st = 0; st < C::NSTG; ++st) {
            const int P = C::spass(st), NT = C::nt(P), GK = NT * C::TT;
            { const int need = (C::cert_next(st) && st + 1 < C::NSTG) ? st + 1 : st; p.waitW[st] = cnt - posW[need]; if (numW[need] != C::ppw(need) || numW[st] != C::ppw(st)) p.left += 10000000;
              if (C::ppw(need) == 0) p.waitW[st] = 63;
              if (C::drain_at(st)) { if (cnt - posE < p.waitW[st]) p.waitW[st] = cnt - posE; if (qlh < qln) p.left += 10000000; } }
            p.waitP[st] = cnt - posP;
            if (st == 1) p.waitPre1 = cnt - C::PRE_VM;
            cnt += C::extra_vm(st);
            for (int g = 0; g < C::NSTG; ++g) if (C::wissue(g) == st) for (int i = 0; i < C::ppw(g); ++i) { qm[qmn++] = OPC(OP_DMS, g * 16 + i); qm[qmn++] = OPC(OP_DML, g * 16 + i); }
            const bool first_of_pass = st == 0 || C::spass(st - 1) != P;
            for (int s = 0; s < C::snks(st); ++s) {
                const int ks = C::sk0(st) + s;
                const int vk = P * C::NKS + ks, NV = C::NKS * C::APASS; const bool apass = !C::A_VIA_LDS && P < C::APASS;
                if (apass) for (int rr = 0; rr < 2; ++rr) if (nexta < NV && nexta <= vk + C::ADX && (st >= C::a_from(nexta / C::NKS))) { for (int t = 0; t < C::TT; ++t) qh[qhn++] = OPC(OP_ALD, nexta * 4 + t); ++nexta; }
                if (apass) { p.waitA[vk] = cnt - posA[vk]; if (posA[vk] == 0 || posA[vk + 1 < NV ? vk + 1 : vk] == 0) p.left += 10000000; }
                if (C::A_VIA_LDS && P == 0) for (int a = 2; a < C::NKS / 2; ++a) if ((a == 2 ? 0 : 2 * a - 4) == ks) for (int i = 0; i < 8; ++i) { qm[qmn++] = OPC(OP_AMS, a * 16 + i); qm[qmn++] = OPC(OP_AML, a * 16 + i); }
                const bool lastk = s + 1 == C::snks(st);
                const bool hasnext = !lastk || (st + 1 < C::NSTG && C::cert_next(st));
                const int NTn = lastk ? (st + 1 < C::NSTG ? C::nt(C::spass(st + 1)) : 0) : NT;
                const int nrd = hasnext ? 2 * NTn : 0;
                const bool ard = C::A_VIA_LDS && P == 0 && ks + 1 < C::NKS && (ks + 1) % 2 == 0;
                const bool dense = GK < 16 || C::TT < 4 || (P == 0 && (!C::WB1 || C::A_VIA_LDS) && C::APASS > 0);
                const bool expl = C::TT == 4 && NT == 4 && prev_plain && nrd == 8 && !ard;
                for (int m = 0; m < GK; ++m, ++G) {
                    int cap = 2, n = 0;
                    p.lw[G] = -1;
                    if (expl) { if (m == 0) { p.lw[G] = 4; cap -= 1; } if (m == 8) { p.lw[G] = 8; cap -= 1; } }
                    else if (C::WB1 && NT == 9 && !C::A_VIA_LDS && hasnext && !(s == 0 && (st == 0 || !C::cert_next(st - 1)))) {
                        if (m % (2 * C::TT) == 0) { p.lw[G] = m / C::TT == 8 ? 14 : 12; cap -= 1; } }
                    else if (m % C::TT == 0) cap -= 1;
                    if (apass && m == 0) cap -= 1;
                    if (C::WB1) {
                        const int cprev = m / C::TT - 1, hf = m % C::TT;
                        if (hf < 2 && cprev >= 0 && cprev < NTn - 1 && hasnext) { p.op[G][n++] = OPC(OP_RD, 2 * cprev + hf); cap -= 1; }
                        if (hf < 2 && cprev < 0 && !(s == 0 && (st == 0 || !C::cert_next(st - 1)))) { p.op[G][n++] = OPC(OP_RD, 4096 + 2 * (NT - 1) + hf); cap -= 1; }
                    } else
                    for (int r = 0; r < nrd; ++r) { const int at = (C::TT == 4 && nrd <= GK) ? r : r * GK / nrd; if (at == m) { p.op[G][n++] = OPC(OP_RD, r); cap -= 1; } }
                    if (ard && m % 4 == 2) {
                        if (m == 2) { p.op[G][n++] = OPC(OP_AWAIT, (ks + 1) / 2); p.warg[G] = cnt - posA[(ks + 1) / 2]; cap -= 1; if (numA[(ks + 1) / 2] != 8) p.left += 10000000; }
                        p.op[G][n++] = OPC(OP_ARD, (ks + 1 + (m >> 4)) * 4 + ((m >> 2) & 3)); cap -= 1;
                    }
                    if (s == C::snks(st) - 2 && st + 1 < C::NSTG && m >= GK - C::nb(st + 1) && m < GK) { p.op[G][n++] = OPC(OP_SETB, (st + 1) * 4 + (m - (GK - C::nb(st + 1)))); cap -= 1; }
                    if (s == 0 && m == 1 && C::poll_at(st + 1)) { p.op[G][n++] = OPC(OP_PL, st + 1); cap -= 4; ++cnt; posP = cnt; }
                    if (first_of_pass && P >= 1 && s == 0 && m == (GK > 8 ? 8 : GK - 1)) {
                        for (int i = 0; i < C::nepi(P - 1); ++i) ql[qln++] = OPC(OP_EPI, (P - 1) * 256 + i);
                        epi_num = C::NKS * GK * C::EPI_SPAN / 16; epi_den = C::nepi(P - 1); epi_g0 = G; epi_done = 0; }
                    const int big = dense ? 1 : 2;
                    for (;;) {
                        if (n >= 5) break;
                        if (qhh < qhn && cap >= big) { const int o = qh[qhh++]; p.op[G][n++] = o; cap -= 4; ++cnt; posA[(o & 65535) >> 2] = cnt; continue; }
                        if (qmh < qmn) { const int o = qm[qmh], k = o >> 16; const bool sc = k == OP_DMS || k == OP_AMS;
                            if (cap >= big) { p.op[G][n++] = o; ++qmh; cap -= sc ? 2 : 4;
                                if (!sc) { ++cnt; if (k == OP_DML) { posW[(o & 65535) >> 4] = cnt; ++numW[(o & 65535) >> 4]; } else { posA[(o & 65535) >> 4] = cnt; ++numA[(o & 65535) >> 4]; } } continue; } }
                        if (qlh < qln && (G - epi_g0) * epi_den >= epi_done * epi_num && st >= C::epi_stage((ql[qlh] & 65535) >> 8, ql[qlh] & 255)) { const int o = ql[qlh], e = o & 255, c = C::epi_cost((o & 65535) >> 8, e);
                            const bool behind = (G - epi_g0) * epi_den >= (epi_done + 3) * epi_num;
                            const bool far = (G - epi_g0) * epi_den >= (epi_done + 8) * epi_num;
                            if (cap >= (far ? 0 : (behind ? 1 : (c < big ? c : big)))) { p.op[G][n++] = o; ++qlh; cap -= c; cnt += C::epi_vm((o & 65535) >> 8, e); if (C::epi_vm((o & 65535) >> 8, e)) posE = cnt; ++epi_done; continue; } }
                        break;
                    }
                    p.nop[G] = n;
                }
                prev_plain = C::TT == 4 && NT == 4 && nrd == 8 && !ard;
            }
            if (!C::cert_next(st)) prev_plain = false;
            if (st + 1 < C::NSTG && C::spass(st + 1) != P && qlh < qln) p.left += 100000;
        }
        p.left += (qhn - qhh) + (qln - qlh) + (qmn - qmh);
        return p;
    }
    static constexpr Plan PL = mk();
    static_assert(PL.left == 0, "planner could not place every operation");

    template <int ST, int S, int O, int WA> static DI void doop(Ctx& c) {
        constexpr int k = O >> 16, a = O & 65535;
        if constexpr (k == OP_RD && a >= 4096) C::template readW<ST, S>(c, 0, (a - 4096) >> 1, a & 1);
        else if constexpr (k == OP_RD) { constexpr bool lastk = S + 1 == C::snks(ST); constexpr int ksg = C::kseq(ST, S) + 1;
            C::template readW<(lastk ? ST + 1 : ST), (lastk ? 0 : S + 1)>(c, C::WB1 ? 0 : (ksg & 1), a >> 1, a & 1); }
        else if constexpr (k == OP_DMS) C::template dmas<(a >> 4), (a & 15)>(c);
        else if constexpr (k == OP_DML) C::template dmal<(a >> 4), (a & 15)>(c);
        else if constexpr (k == OP_ALD) C::template aload<(a >> 2), (a & 3)>(c);
        else if constexpr (k == OP_EPI) C::template epi<(a >> 8), (a & 255)>(c);
        else if constexpr (k == OP_SETB) C::template setb<(a >> 2), (a & 3)>(c);
        else if constexpr (k == OP_AMS) C::template admas<(a >> 4), (a & 15)>(c);
        else if constexpr (k == OP_AML) C::template admal<(a >> 4), (a & 15)>(c);
        else if constexpr (k == OP_ARD) C::template aread<(a >> 2), (a & 3)>(c);
        else if constexpr (k == OP_AWAIT) wait_vm<WA>();
        else if constexpr (k == OP_PL) poll_issue(c.y, C::template poll_idx<a>(c), (c.lane16 >> 1) & (C::POLL_NEED * 8 - 8), c.pv);
    }
    template <int ST, int M> static DI void gap(Ctx& c) {
        constexpr int P = C::spass(ST), NT = C::nt(P), GK = NT * C::TT, s = M / GK, m = M % GK, cc = m / C::TT, t = m % C::TT, ks = C::sk0(ST) + s, G = gstart(ST) + M;
        if constexpr (!C::A_VIA_LDS && P < C::APASS && m == 0) wait_vm<PL.waitA[P < C::APASS ? P * C::NKS + ks : 0]>();
        if constexpr (PL.lw[G] >= 0) wait_lgkm_known<(PL.lw[G] >= 0 ? PL.lw[G] : 0)>();
        C::template mfma<P, ks, cc, t>(c, cat(c.w[C::WB1 ? 0 : (C::kseq(ST, s) & 1)][cc][0], c.w[C::WB1 ? 0 : (C::kseq(ST, s) & 1)][cc][1]));
        SBAR();
        if constexpr (PL.nop[G] > 0) doop<ST, s, PL.op[G][0], PL.warg[G]>(c);
        if constexpr (PL.nop[G] > 1) doop<ST, s, PL.op[G][1], PL.warg[G]>(c);
        if constexpr (PL.nop[G] > 2) doop<ST, s, PL.op[G][2], PL.warg[G]>(c);
        if constexpr (PL.nop[G] > 3) doop<ST, s, PL.op[G][3], PL.warg[G]>(c);
        if constexpr (PL.nop[G] > 4) doop<ST, s, PL.op[G][4], PL.warg[G]>(c);
        SBAR();
    }
    template <int ST, int R> static DI void rd0(Ctx& c) { C::template readW<ST, 0>(c, C::WB1 ? 0 : (C::kseq(ST, 0) & 1), R >> 1, R & 1); }
    template <int ST, int... Rs> static DI void rd0s(Ctx& c, iseq<Rs...>) { (rd0<ST, Rs>(c), ...); }
    template <int... Bs> static DI void setb0(Ctx& c, iseq<Bs...>) { (C::template setb<0, Bs>(c), ...); }
    template <int ST, int... Ms> static DI void stage_(Ctx& c, Stamp& sp, iseq<Ms...>) {
        if constexpr (C::stamp_at(ST)) STAMP_AT(sp);
        if constexpr (ST > 0 && C::spass(ST) != C::spass(ST > 0 ? ST - 1 : 0)) C::template pass_end<C::spass(ST) - 1>(c);
        if constexpr (C::bar_lgkm(ST)) wait_lgkm0();
        if constexpr (C::poll_at(ST)) { asm volatile("s_waitcnt vmcnt(%1)" : "+v"(c.pv) : "i"(PL.waitP[ST] > 63 ? 63 : PL.waitP[ST]) : "memory"); poll_check(c.y, C::template poll_idx<ST>(c), C::POLL_NEED, c.pv); }
        if constexpr (C::need_bar(ST)) wait_vm_bar<PL.waitW[ST]>();
        C::template post_barrier<ST>(c);
        SBAR();
        if constexpr (ST == 0 || !C::cert_next(ST > 0 ? ST - 1 : 0)) { rd0s<ST>(c, mkseq<2 * C::nt(C::spass(ST))>{}); SBAR(); }
        (gap<ST, Ms>(c), ...);
    }
    template <int... Ss> static DI void stages_(Ctx& c, Stamp& sp, iseq<Ss...>) { (stage_<Ss>(c, sp, mkseq<gaps(Ss)>{}), ...); }
    template <int G, int... Is> static DI void prow1(Ctx& c, iseq<Is...>) { ((C::template dmas<G, Is>(c), C::template dmal<G, Is>(c)), ...); }
    template <int G> static DI void prow0(Ctx& c) { if constexpr (C::wissue(G) < 0) prow1<G>(c, mkseq<C::ppw(G)>{}); }
    template <int... Gs> static DI void prow(Ctx& c, iseq<Gs...>) { (prow0<Gs>(c), ...); }
    template <int... Is> static DI void proa(Ctx& c, iseq<Is...>) { (C::template aload<Is / C::TT, Is % C::TT>(c), ...); }
    template <int A, int... Is> static DI void proa0(Ctx& c, iseq<Is...>) { ((C::template admas<A, Is>(c), C::template admal<A, Is>(c)), ...); }
    static DI void prologueW(Ctx& c) { if constexpr (C::A_VIA_LDS) proa0<0>(c, mkseq<8>{}); prow(c, mkseq<C::NSTG>{}); if constexpr (C::A_VIA_LDS) proa0<1>(c, mkseq<8>{}); SBAR(); }
    template <int G0, int... Gs> static DI void prowr(Ctx& c, iseq<Gs...>) { (prow0<G0 + Gs>(c), ...); }
    template <int G0, int G1> static DI void prologueWr(Ctx& c) { prowr<G0>(c, mkseq<G1 - G0>{}); SBAR(); }
    static DI void prologueA(Ctx& c) { proa(c, mkseq<C::AD * C::TT>{}); setb0(c, mkseq<C::nb(0)>{}); SBAR(); }
    static DI void run(Ctx& c, Stamp& sp) { stages_(c, sp, mkseq<C::NSTG>{}); asm volatile("s_nop 7" ::: "memory"); SBAR(); }
    template <int P, int... Es> static DI void epi_all_(Ctx& c, iseq<Es...>) { (C::template epi<P, Es>(c), ...); }
    template <int P> static DI void epi_all(Ctx& c) { epi_all_<P>(c, mkseq<C::nepi(P)>{}); }
};

#define WU_N 0
#define WU_STRIDE 128
#define IN_WU 0
#define WQ_AT 5
#define LN_LATE 0
#define WUV_AT 2
#define QPRE 8
#define COH_A true
#define DPRE 3
#define CODE_WU 1
extern "C" __global__ void k_first() {}
struct CfgBase {
    static constexpr int APASS = 1, EPI_SPAN = 14;
    static constexpr int a_from(int) { return 0; }
    static constexpr int epi_stage(int, int) { return 0; }
    static constexpr int extra_vm(int) { return 0; }
    static constexpr bool drain_at(int) { return false; }
    static constexpr bool need_bar(int) { return true; }
    static constexpr bool poll_at(int) { return false; }
    template <int P, class X> static DI void pass_end(X&) {}
    template <int ST, class X> static DI void post_barrier(X&) {}
};
struct CfgKV : CfgBase {
    static constexpr int TT = 4, NKS = 16, NSTG = 16, AD = 0, ADX = 0, PRE_VM = 6, POST_PRO_VM = (LN_LATE ? 0 : 5) + CODE_WU + IN_WU; static constexpr bool A_VIA_LDS = true, WB1 = true;
    static constexpr int spass(int st) { return st < 8 ? 0 : (st < 12 ? 1 : 2); }
    static constexpr int sk0(int st) { return st < 8 ? 2 * st : 4 * (st & 3); }
    static constexpr int snks(int st) { return st < 8 ? 2 : 4; }
    static constexpr int nt(int p) { return p == 0 ? 8 : 4; }
    static constexpr int kseq(int, int) { return 0; }
    static constexpr int nb(int) { return 4; }
    static constexpr int ppw(int) { return 4; }
    static constexpr int wslot(int g) { return g < 11 ? 6 + (g & 3) : g - 11; }
    static constexpr int wissue(int g) { return g < 2 ? -1 : (g < 4 ? 0 : (g < 11 ? g - 3 : (g < 13 ? 8 : (g < 15 ? 9 : 10)))); }
    static constexpr bool cert_next(int st) { return st >= 1; }
    static constexpr bool bar_lgkm(int) { return false; }
    static constexpr bool stamp_at(int st) { return st < 8 ? (st & 1) == 0 : (st & 3) == 0; }
    static constexpr int NWU = WU_N;
    static constexpr int nepi(int p) { return p == 0 ? 80 : (p == 1 ? NWU + 40 + 4 * QPRE : 40); }
    static constexpr int l1(int e) { if (e < NWU) return 256 + e; e -= NWU; if (e < 10) return e; e -= 10;
        for (int s = 0; s < 3; ++s) { const int nq = 4 * QPRE - 16 * s < 0 ? 0 : (4 * QPRE - 16 * s > 16 ? 16 : 4 * QPRE - 16 * s); if (e < 10) return 10 + 10 * s + e; e -= 10; if (e < nq) return 512 + 16 * s + e; e -= nq; } return 0; }
    static constexpr int eop(int p, int e) { return p == 1 ? l1(e) : e; }
    static constexpr int epi_stage(int p, int e) { return p == 1 && eop(p, e) >= 512 ? 13 + (eop(p, e) - 512) / 16 : 0; }
    static constexpr int epi_cost(int p, int e) { return eop(p, e) >= 256 || eop(p, e) % 5 == 4 ? 4 : 1; }
    static constexpr int epi_vm(int p, int e) { return epi_cost(p, e) == 4; }
    struct Ctx {
        bf16x8 af[4][16]; f32x4 acc[8][4], acc2[4][4]; s16x4 w[1][8][2]; u32x4 eo;
        lds_cptr cb[2][4], ab[2];
        unsigned mA, mW, dso, voffA, voffW[2], lane16; rsrc_t rsC, rsWk, rsWv, rsK, rsV; unsigned sA, sW0, sW1, sK, sV;
        unsigned pf, pfo; rsrc_t rsP[2]; Sync y; unsigned xcnt; unsigned long long pv; u32x2 xv[5]; unsigned row; bf16x8 (*qa)[18]; rsrc_t rsX; unsigned sX;
    };
    static constexpr bool poll_at(int st) { return st == 13; }
    static constexpr unsigned POLL_NEED = 32;
    template <int ST> static DI unsigned poll_idx(Ctx& c) { return c.xcnt; }
    static DI void ln1(Ctx& c);
    static constexpr int extra_vm(int st) { return (st == WUV_AT) + (st == WQ_AT) + (LN_LATE && st == 8 ? 5 : 0); }
    template <int ST> static DI void post_barrier(Ctx& c) {
        if constexpr (LN_LATE && ST == 8) ln1(c);
        if constexpr (ST == (LN_LATE ? 10 : 1)) signal(c.y, CNT_X + (blockIdx.x >> 5), blockIdx.x & 31);
        if constexpr (ST == WUV_AT) asm volatile("buffer_load_dword %0, %1, %2, 0 offen" : "+v"(c.pf) : "v"(c.pfo), "s"(c.rsWv) : "memory");
        if constexpr (ST == WQ_AT) asm volatile("buffer_load_dword %0, %1, %2, 0 offen" : "+v"(c.pf) : "v"(c.pfo), "s"(c.rsP[0]) : "memory"); }
    template <int A, int I> static DI void admas(Ctx& c) { dma_s<(A % 3) * 8192 + I * 1024, 8 * I * 1024 + A * 128>(c.dso, c.mA, c.sA); }
    template <int A, int I> static DI void admal(Ctx& c) { dma_l(c.voffA, c.rsC, c.dso); }
    template <int KS, int T> static DI void aread(Ctx& c) { c.af[T][KS] = *(const LAS bf16x8*)(c.ab[KS & 1] + ((KS >> 1) % 3) * 8192 + T * 2048); }
    template <int G, int I> static DI void dmas(Ctx& c) {
        if constexpr (G < 8) dma_s<wslot(G) * 16384 + I * 1024, (64 * G + 8 * I) * 8192>(c.dso, c.mW, c.sW0);
        else dma_s<wslot(G) * 16384 + I * 1024, (128 * (G & 3) + 8 * I) * 8192 + (G >= 12 ? 128 : 0)>(c.dso, c.mW, c.sW1); }
    template <int G, int I> static DI void dmal(Ctx& c) { dma_l(c.voffW[I & 1], G < 8 ? c.rsWk : c.rsWv, c.dso); }
    template <int G, int S> static DI void readW(Ctx& c, int, int cc, int half) { c.w[0][cc][half] = vtr(c.cb[G & 1][cc & 3] + (cc >> 2) * 8192 + S * 4096 + half * 512); }
    template <int G, int I> static DI void setb(Ctx& c) { if constexpr (G > 0) { c.cb[G & 1][I] = c.cb[(G - 1) & 1][I] + (wslot(G) - wslot(G > 0 ? G - 1 : 0)) * 16384; asm volatile("" : "+v"(c.cb[G & 1][I])); } }
    template <int KS, int T> static DI void aload(Ctx&) {}
    template <int P, int KS, int CC, int T> static DI void mfma(Ctx& c, const bf16x8& wf) {
        if constexpr (P == 0) mf_va<KS == 0>(c.acc[CC][T], wf, c.af[T][KS]); else mf_av<KS == 0>(P == 1 ? c.acc2[CC & 3][T] : c.acc[CC][T], c.af[T][KS], wf); }
    template <int KS, int T> static DI void qload(Ctx& c) { ldfr<(KS & 3) * 1024, (KS < 16), COH_A>(c.qa[T][KS], c.lane16, c.rsX, c.sX + (T * 72 + (KS & ~3)) * 1024); }
    template <int P, int I> static DI void epi(Ctx& c) {
        constexpr int o = eop(P, I);
        if constexpr (o >= 512) qload<((o - 512) >> 2), ((o - 512) & 3)>(c);
        else if constexpr (o >= 256) asm volatile("buffer_load_dword %0, %1, %2, 0 offen" : "+v"(c.pf) : "v"(c.pfo), "s"(c.rsP[(o - 256) & 1]) : "memory");
        else {
            constexpr int E = o / 5, ph = o % 5;
            if constexpr (ph < 4) {
                const f32x4 x = P == 0 ? c.acc[2 * (E & 3) + (ph >> 1)][(E >> 2) & 3] : (P == 1 ? c.acc2[E & 3][2 * ((E >> 2) & 1) + (ph >> 1)] : c.acc[E & 3][2 * ((E >> 2) & 1) + (ph >> 1)]);
                unsigned wd = cvtpk(x[2 * (ph & 1)], x[2 * (ph & 1) + 1]); asm volatile("" : "+v"(wd)); c.eo[ph] = wd;
            } else if constexpr (P == 0) __builtin_amdgcn_raw_buffer_store_b128(c.eo, c.rsK, c.lane16, c.sK + (unsigned)(E * 1024), SC1);
            else __builtin_amdgcn_raw_buffer_store_b128(c.eo, c.rsV, c.lane16, c.sV + (unsigned)(((E >> 2) * 8 + 4 * (P - 1) + (E & 3)) * 1024), SC1);
        }
    }
};
DI void code_touch(unsigned part, unsigned lane, unsigned& sink) {
    unsigned lo0, hi0, lo1, hi1;
    asm volatile("s_getpc_b64 vcc\n\ts_add_u32 %0, vcc_lo, k_first@rel32@lo+4\n\ts_addc_u32 %1, vcc_hi, k_first@rel32@hi+12\n\t"
                 "s_getpc_b64 vcc\n\ts_add_u32 %2, vcc_lo, k_last@rel32@lo+4\n\ts_addc_u32 %3, vcc_hi, k_last@rel32@hi+12"
                 : "=&s"(lo0), "=&s"(hi0), "=&s"(lo1), "=&s"(hi1) :: "vcc", "scc");
    const unsigned long long b = ((unsigned long long)hi0 << 32) | lo0, e = ((unsigned long long)hi1 << 32) | lo1;
    const unsigned bytes = (e > b && e - b < (1u << 20)) ? (unsigned)(e - b) : 0u;
    const rsrc_t rs = mkrsrc((const void*)b, bytes);
    const unsigned vo = (part * 64 + lane) * 64;
#pragma unroll
    for (int i = 0; i < CODE_WU; ++i) asm volatile("buffer_load_dword %0, %1, %2, %3 offen" : "+v"(sink) : "v"(vo), "s"(rs), "s"(i * 524288u) : "memory");
}
template <int... Is> DI void kv_aread0(CfgKV::Ctx& c, iseq<Is...>) { (CfgKV::aread<(Is >> 2), (Is & 3)>(c), ...); }

DI void CfgKV::ln1(Ctx& c) {
    asm volatile("" : "+v"(c.xv[0]), "+v"(c.xv[1]), "+v"(c.xv[2]), "+v"(c.xv[3]), "+v"(c.xv[4]), "+v"(c.pv) :: "memory");
    c.y.tag = (((unsigned long long)rfl((unsigned)(c.pv >> 32)) << 32) | rfl((unsigned)c.pv)) + 1ull;
    const unsigned row = c.row, li = (c.lane16 >> 4) & 31; char* ws = c.y.ws;
    {
        float v[20]; const bool has4 = li < 16;
#pragma unroll
        for (int m = 0; m < 5; ++m) { v[4 * m] = bflo(c.xv[m][0]); v[4 * m + 1] = bfhi(c.xv[m][0]); v[4 * m + 2] = bflo(c.xv[m][1]); v[4 * m + 3] = bfhi(c.xv[m][1]); }
        float s = 0;
#pragma unroll
        for (int e = 0; e < 16; ++e) s += v[e];
        if (has4) s += (v[16] + v[17]) + (v[18] + v[19]);
        const float mean = sum32(s) * (1.0f / DM);
        float qq = 0;
#pragma unroll
        for (int e = 0; e < 20; ++e) { v[e] -= mean; if (e < 16 || has4) qq += v[e] * v[e]; }
        const float rstd = __builtin_amdgcn_rsqf(sum32(qq) * (1.0f / DM) + 1e-5f) * QSCALE;
        const rsrc_t rsX = c.rsX;
#pragma unroll
        for (int m = 0; m < 5; ++m) {
            const unsigned qd = m < 4 ? li + 32 * m : 128 + (li & 15);
            const unsigned off = (((row >> 4) * 18 + (qd >> 3)) * 64 + (qd & 3) * 16 + (row & 15)) * 16 + ((qd >> 2) & 1) * 8;
            const u32x2 o = {cvtpk(v[4 * m] * rstd, v[4 * m + 1] * rstd), cvtpk(v[4 * m + 2] * rstd, v[4 * m + 3] * rstd)};
            __builtin_amdgcn_raw_buffer_store_b64(o, rsX, off, 0, SC1);
        }
    }
    SBAR();
}
template <class F> DI void kv_phase(F&& before_drain, const unsigned short* x, const unsigned short* ckv, const unsigned short* wuk, const unsigned short* wuv, char* ws,
                 const unsigned short* wq, const unsigned short* wo, char* lds, Sync& y, unsigned qblk, bf16x8 (*qa)[18], Stamp& sp) {
    using E = Eng<CfgKV>;
    const unsigned lane = fresh_lane(), r = lane & 15, g = lane >> 4, q = (lane & 15) >> 2, p = lane & 3;
    CfgKV::Ctx c; const unsigned wid = y.wid;
    const unsigned lds0 = (unsigned)(uintptr_t)lds; c.lane16 = lane * 16; c.xcnt = CNT_X + qblk; c.qa = qa; c.rsX = mkrsrc(ws + OFF_XN, (unsigned)SZ_XN); c.sX = (qblk * 16 + wid) * 18 * 1024;
    const unsigned bid = blockIdx.x, xcd = bid & 7, idx = bid >> 3, rt = 4 * (xcd & 1) + (idx & 3), h = 8 * (xcd >> 1) + (idx >> 2);
    const unsigned T0 = rt * 256 + wid * 64;
    c.rsC = mkrsrc(ckv, S_ * DC * 2); c.rsWk = mkrsrc(wuk, DC * 4096 * 2); c.rsWv = mkrsrc(wuv, DC * 4096 * 2);
    c.rsK = mkrsrc(ws + OFF_KF, (unsigned)SZ_KF); c.rsV = mkrsrc(ws + OFF_VF, (unsigned)SZ_VF);
    c.mA = lds0 + wid * 24576; c.mW = lds0 + wid * 4096;
    c.rsP[0] = mkrsrc(wq, DM * 4096 * 2); c.rsP[1] = mkrsrc(wo, DM * 4096 * 2);
    c.pfo = ((bid * 4 + wid) * 64 + lane) * WU_STRIDE; c.pf = 0u;
    c.sA = T0 * 1024; c.sW0 = (wid & 1) * 32 * 8192 + h * 256 + (wid >> 1) * 128; c.sW1 = wid * 32 * 8192 + h * 256;
    c.sK = (h * 128 + T0 / 16) * 4096; c.sV = (h * 64 + T0 / 32) * 8192;
    c.voffA = (lane >> 3) * 1024 + (((lane & 7) ^ (lane >> 3)) << 4);
#pragma unroll
    for (int i = 0; i < 2; ++i) { const unsigned r8 = lane >> 3, f = ((r8 >> 1) & 1) | (i << 1); c.voffW[i] = r8 * 8192 + (((lane & 7) ^ (f << 1)) << 4); }
    { const unsigned f = ((q >> 1) & 1) | ((g & 1) << 1);
#pragma unroll
      for (int cc = 0; cc < 4; ++cc) c.cb[0][cc] = (lds_cptr)lds + CfgKV::wslot(0) * 16384 + (8 * g + q) * 128 + (((cc ^ f) << 5) + 8 * p); }
#pragma unroll
    for (int T = 0; T < 2; ++T) c.ab[T] = (lds_cptr)lds + wid * 24576 + r * 128 + (((4 * T + g) ^ (r & 7)) << 4);
    const unsigned row = bid * 8 + wid * 2 + (lane >> 5), li = lane & 31;
    c.row = row; c.y = y;
    { const char* xp = (const char*)x + row * (DM * 2) + li * 8; const char* xp4 = (const char*)x + row * (DM * 2) + (128 + (li & 15)) * 8;
      asm volatile("global_load_dwordx2 %0, %1, off" : "=v"(c.xv[0]) : "v"(xp) : "memory");
      asm volatile("global_load_dwordx2 %0, %1, off offset:256" : "=v"(c.xv[1]) : "v"(xp) : "memory");
      asm volatile("global_load_dwordx2 %0, %1, off offset:512" : "=v"(c.xv[2]) : "v"(xp) : "memory");
      asm volatile("global_load_dwordx2 %0, %1, off offset:768" : "=v"(c.xv[3]) : "v"(xp) : "memory");
      asm volatile("global_load_dwordx2 %0, %1, off" : "=v"(c.xv[4]) : "v"(xp4) : "memory"); }
    asm volatile("global_load_dwordx2 %0, %1, %2 sc1" : "=v"(c.pv) : "v"(0u), "s"(ws + OFF_CNT + OFF_EPOCH) : "memory");
    E::prologueW(c); E::prologueA(c);
    code_touch(idx * 4 + wid, lane, c.pf);
    if constexpr (IN_WU >= 1) asm volatile("buffer_load_dword %0, %1, %2, 0 offen" : "+v"(c.pf) : "v"(c.pfo), "s"(c.rsWk) : "memory");
    if constexpr (IN_WU >= 2) asm volatile("buffer_load_dword %0, %1, %2, 0 offen" : "+v"(c.pf) : "v"(c.pfo), "s"(c.rsC) : "memory");
    if constexpr (!LN_LATE) { wait_vm<E::PL.waitPre + CODE_WU + IN_WU>(); CfgKV::ln1(c); }
    STAMP_AT(sp);
    wait_vm<E::PL.waitA0>();
    kv_aread0(c, mkseq<8>{});
    SBAR();
    E::run(c, sp);
    STAMP_AT(sp);
    E::epi_all<2>(c);
    before_drain();
    barrier_all();
    asm volatile("" :: "v"(c.pf));
    signal(c.y, CNT_K + h, rt);
    y.tag = c.y.tag;
}

struct CfgGU : CfgBase {
    static constexpr int TT = 4, NKS = 18, NSTG = 15, AD = 3, ADX = 7, PRE_VM = 0, POST_PRO_VM = 0, NSLOT = 6, STB = 24576; static constexpr bool A_VIA_LDS = false, WB1 = false;
    static constexpr int spass(int st) { return st / 3; }
    static constexpr int sk0(int st) { return 6 * (st % 3); }
    static constexpr int snks(int) { return 6; }
    static constexpr int nt(int p) { return p < 4 ? 4 : 2; }
    static constexpr int kseq(int st, int s) { return st * 6 + s; }
    static constexpr int nb(int st) { return st < 12 ? 2 : 1; }
    static constexpr int ppw(int) { return 6; }
    static constexpr int wissue(int g) { return g < 3 ? -1 : (g < 4 ? 1 : (g < 6 ? 2 : g - 3)); }
    static constexpr bool cert_next(int) { return true; }
    static constexpr bool bar_lgkm(int) { return false; }
    static constexpr bool stamp_at(int st) { return st < 3 || st % 3 == 0; }
    static constexpr int nepi(int) { return 4 * 53; }
    static constexpr int epi_cost(int, int e) { const int k = e % 53; return k < 48 ? ((k % 6 == 1 || k % 6 == 4) ? 2 : 1) : (k < 52 ? 1 : 4); }
    static constexpr int epi_vm(int, int e) { return e % 53 == 52; }
    struct Ctx {
        bf16x8 af[4][18]; f32x4 acc[2][4][4]; s16x4 w[2][4][2]; float ev[8], et[2]; u32x4 eo;
        lds_cptr rb[2], rbs, cb[2][2];
        unsigned mW, dso, voffW, lane16; rsrc_t rsG, rsA, rsO; unsigned sW[5], sO[5], sA;
    };
    template <int G, int I> static DI void dmas(Ctx& c) { dma_s<(G % NSLOT) * STB + I * 1024, (192 * (G % 3) + 16 * I) * (FF * 2)>(c.dso, c.mW, c.sW[G / 3]); }
    template <int G, int I> static DI void dmal(Ctx& c) { dma_l(c.voffW, c.rsG, c.dso); }
    template <int G, int S> static DI void readW(Ctx& c, int buf, int cc, int half) { c.w[buf][cc][half] = vtr(c.cb[G & 1][cc >> 1] + (cc & 1) * 12288 + S * 2048 + half * 1024); }
    template <int G, int I> static DI void setb(Ctx& c) { c.cb[G & 1][I] = (G >= 12 ? c.rbs : c.rb[I]) + (G % NSLOT) * STB; asm volatile("" : "+v"(c.cb[G & 1][I])); }
    template <int KS, int T> static DI void aload(Ctx& c) { ldfr<(KS & 3) * 1024, (KS < 16), COH_A>(c.af[T][KS], c.lane16, c.rsA, c.sA + (T * 18 + (KS & ~3)) * 1024); }
    template <int P, int KS, int CC, int T> static DI void mfma(Ctx& c, const bf16x8& wf) {
        if constexpr (KS < 16) mf_va<KS == 0>(c.acc[P & 1][CC][T], wf, c.af[T][KS]); else mf_vv<false>(c.acc[P & 1][CC][T], wf, c.af[T][KS]); }
    template <int P, int I> static DI void epi(Ctx& c) {
        constexpr int T = I / 53, k = I % 53;
        if constexpr (k < 48) {
            constexpr int V = k / 6, ph = k % 6, hf = V >> 2, e = V & 3;
            const float g = c.acc[P & 1][2 * hf][T][e], u = c.acc[P & 1][2 * hf + 1][T][e];
            float& t = c.et[V & 1];
            if constexpr (ph == 0) { t = g * -1.4426950408889634f; asm volatile("" : "+v"(t)); }
            else if constexpr (ph == 1) { t = __builtin_amdgcn_exp2f(t); asm volatile("" : "+v"(t)); }
            else if constexpr (ph == 2) { c.ev[V] = g * u; asm volatile("" : "+v"(c.ev[V])); }
            else if constexpr (ph == 3) { t = 1.0f + t; asm volatile("" : "+v"(t)); }
            else if constexpr (ph == 4) { t = __builtin_amdgcn_rcpf(t); asm volatile("" : "+v"(t)); }
            else { if constexpr (V >= 1) { c.ev[V - 1] *= c.et[(V - 1) & 1]; asm volatile("" : "+v"(c.ev[V - 1])); } }
        } else if constexpr (k < 52) { constexpr int j = k - 48;
            if constexpr (j == 3) c.ev[7] *= c.et[1];
            c.eo[j] = cvtpk(c.ev[2 * j], c.ev[2 * j + 1]); asm volatile("" : "+v"(c.eo)); }
        else __builtin_amdgcn_raw_buffer_store_b128(c.eo, c.rsO, c.lane16, c.sO[P] + (unsigned)(T * 144 * 1024), SC1);
    }
    static DI void setup(Ctx& c, const unsigned short* wg, const unsigned short* wu, char* ws, char* lds, unsigned wid, unsigned rt, unsigned cg) {
        const unsigned lane = fresh_lane(), g = lane >> 4, q = (lane & 15) >> 2, p = lane & 3;
        c.mW = (unsigned)(uintptr_t)lds + wid * 6144; c.lane16 = lane * 16;
        const unsigned odd = cg & 1, f0 = cg * 144, tt0 = rt * 16 + wid * 4;
        c.rsG = mkrsrc(wid < 2 ? wg : wu, DM * FF * 2);
        c.rsA = mkrsrc(ws + OFF_HN, (unsigned)SZ_HN); c.rsO = mkrsrc(ws + OFF_ACT, (unsigned)SZ_ACT);
#pragma unroll
        for (int pp = 0; pp < 5; ++pp) {
            const unsigned fa = pp < 4 ? f0 + odd * 16 + 32 * pp : (odd ? f0 - 16 : f0 + 128);
            c.sW[pp] = (wid & 1) * 96 * (FF * 2) + fa * 2;
            c.sO[pp] = (tt0 * 144 + fa / 32) * 1024;
        }
        c.voffW = (lane >> 2) * (FF * 2) + (((lane & 3) ^ (((lane >> 4) & 1) << 1)) << 4);
#pragma unroll
        for (int u = 0; u < 2; ++u) c.rb[u] = (lds_cptr)lds + (4 * g + q) * 64 + ((u ^ (g & 1)) << 5) + 8 * p;
        c.rbs = (lds_cptr)lds + (4 * g + q) * 64 + ((odd ^ (g & 1)) << 5) + 8 * p;
        c.sA = tt0 * 18 * 1024;
    }
    static DI void tail(Ctx& c, unsigned odd) {
#pragma unroll
        for (int t = 0; t < 4; ++t) {
            float v[4];
#pragma unroll
            for (int e = 0; e < 4; ++e) { const float g = c.acc[0][0][t][e], u = c.acc[0][1][t][e]; v[e] = g * u * __builtin_amdgcn_rcpf(1.0f + __builtin_amdgcn_exp2f(g * -1.4426950408889634f)); }
            const u32x2 o = {cvtpk(v[0], v[1]), cvtpk(v[2], v[3])};
            __builtin_amdgcn_raw_buffer_store_b64(o, c.rsO, c.lane16 + odd * 8, c.sO[4] + (unsigned)(t * 144 * 1024), SC1);
        }
    }
};

template <int NKS_> struct CfgN : CfgBase {
    static constexpr int TT = 4, NKS = NKS_, NSTG = NKS / 2, AD = 3, ADX = 9, PRE_VM = 0, POST_PRO_VM = 0, NSLOT = 4, STB = 36864; static constexpr bool A_VIA_LDS = false, WB1 = true;
    static constexpr int spass(int) { return 0; }
    static constexpr int sk0(int st) { return 2 * st; }
    static constexpr int snks(int) { return 2; }
    static constexpr int nt(int) { return 9; }
    static constexpr int kseq(int st, int s) { return 2 * st + s; }
    static constexpr int nb(int) { return 1; }
    static constexpr int ppw(int) { return 9; }
    static constexpr int wissue(int g) { return g < DPRE ? -1 : g - 3; }
    static constexpr bool cert_next(int) { return true; }
    static constexpr bool bar_lgkm(int) { return false; }
    static constexpr bool stamp_at(int st) { return st < 2 || st == 4; }
    static constexpr int nepi(int) { return 0; }
    static constexpr int epi_cost(int, int) { return 1; }
    static constexpr int epi_vm(int, int) { return 0; }
    struct Ctx {
        bf16x8 af[4][NKS]; f32x4 acc[9][4]; s16x4 w[1][9][2];
        lds_cptr rb, cb[2];
        unsigned mW, dso, voffW[9], lane16, voffP; rsrc_t rsW, rsA, rsP; unsigned sW, sA, sP;
    };
    template <int G, int I> static DI void dmas(Ctx& c) { dma_s<(G % NSLOT) * STB + I * 1024, 64 * G * 1152>(c.dso, c.mW, c.sW); }
    template <int G, int I> static DI void dmal(Ctx& c) { dma_l(c.voffW[I], c.rsW, c.dso); }
    template <int G, int S> static DI void readW(Ctx& c, int buf, int cc, int half) { c.w[0][cc][half] = vtr(c.cb[G & 1] + S * 9216 + half * 4608 + cc * 32); }
    template <int G, int I> static DI void setb(Ctx& c) { c.cb[G & 1] = c.rb + (G % NSLOT) * STB; asm volatile("" : "+v"(c.cb[G & 1])); }
    template <int KS, int T> static DI void aload(Ctx& c) { ldfr<(KS & 3) * 1024, (KS < 16), COH_A>(c.af[T][KS], c.lane16, c.rsA, c.sA + (T * NKS * 8 + (KS & ~3)) * 1024); }
    template <int P, int KS, int CC, int T> static DI void mfma(Ctx& c, const bf16x8& wf) {
        if constexpr (KS < 16) mf_va<KS == 0>(c.acc[CC][T], wf, c.af[T][KS]); else mf_vv<false>(c.acc[CC][T], wf, c.af[T][KS]); }
    template <int P, int I> static DI void epi(Ctx&) {}
    static DI void setup(Ctx& c, const unsigned short* W, const char* A, unsigned abytes, char* Pp, char* lds, unsigned wid, unsigned rt, unsigned cg, unsigned sp) {
        const unsigned lane = fresh_lane(), g = lane >> 4, q = (lane & 15) >> 2, p = lane & 3, tg = wid & 1, kh = wid >> 1;
        c.mW = (unsigned)(uintptr_t)lds + wid * 9216; c.lane16 = lane * 16;
        c.rsW = mkrsrc(W, NKS * 256 * DM * 2); c.rsA = mkrsrc(A, abytes); c.rsP = mkrsrc(Pp, (unsigned)SZ_PART);
        const unsigned tt0 = rt * 8 + tg * 4;
        c.sW = ((sp * 2 + kh) * NKS * 32 + tg * 32) * 1152 + cg * 288;
        c.sA = (tt0 * NKS * 8 + (sp * 2 + kh) * NKS) * 1024;
        c.sP = ((tt0 >> 1) + kh) * 147456 + sp * 9216 + cg * 2304;
        c.voffP = ((lane & 15) >> 2) * 36864 + g * 64 + (lane & 3) * 16;
#pragma unroll
        for (int b = 0; b < 9; ++b) { const unsigned ci = 64 * b + lane; c.voffW[b] = (ci / 18) * 1152 + (ci % 18) * 16; }
        c.rb = (lds_cptr)lds + kh * 18432 + (4 * g + q) * 288 + 8 * p;
    }
    template <int KH> static DI void tail_(Ctx& c, char* lds, unsigned wid) {
        LAS f32x4* xw = (LAS f32x4*)((lds_cptr)lds + wid * 18432 + c.lane16); const LAS f32x4* xr = (const LAS f32x4*)((lds_cptr)lds + (wid ^ 2) * 18432 + c.lane16);
        asm volatile("" : "+v"(xw), "+v"(xr));
#pragma unroll
        for (int cc = 0; cc < 9; ++cc) { xw[(2 * cc) * 64] = c.acc[cc][2 * (1 - KH)]; xw[(2 * cc + 1) * 64] = c.acc[cc][2 * (1 - KH) + 1]; }
        barrier_lds();
#pragma unroll
        for (int cc = 0; cc < 9; ++cc) {
            const f32x4 x = xr[(2 * cc) * 64] + c.acc[cc][2 * KH], y = xr[(2 * cc + 1) * 64] + c.acc[cc][2 * KH + 1];
            const u32x4 o = {cvtpk(x[0], x[1]), cvtpk(x[2], x[3]), cvtpk(y[0], y[1]), cvtpk(y[2], y[3])};
            __builtin_amdgcn_raw_buffer_store_b128(o, c.rsP, c.voffP, c.sP + (unsigned)(cc * 256), SC1);
        }
    }
    static DI void tail(Ctx& c, char* lds, unsigned wid) {
        barrier_lds();
        if (wid < 2) tail_<0>(c, lds, wid); else tail_<1>(c, lds, wid);
    }
};

template <int NP> struct CfgO {
    static constexpr int TT = 2, NKS = 16, NSTG = 4 * NP, AD = 6, ADX = 14, PRE_VM = 0, POST_PRO_VM = 0, STB = 36864, APASS = NP, EPI_SPAN = 7; static constexpr bool A_VIA_LDS = false, WB1 = true;
    static constexpr int a_from(int p) { return p == 0 ? 0 : 4 * p - 2; }
    static constexpr int epi_stage(int, int) { return 0; }
    static constexpr int extra_vm(int) { return 0; }
    static constexpr int spass(int st) { return st >> 2; }
    static constexpr int sk0(int st) { return 4 * (st & 3); }
    static constexpr int snks(int) { return 4; }
    static constexpr int nt(int) { return 9; }
    static constexpr int kseq(int st, int s) { return 4 * st + s; }
    static constexpr int nb(int) { return 1; }
    static constexpr int ppw(int g) { return g < 4 ? 9 : 0; }
    static constexpr int wissue(int g) { return g < 2 ? -1 : (g < 4 ? g - 2 : 99); }
    static constexpr bool cert_next(int st) { return st > 0; }
    static constexpr bool bar_lgkm(int) { return false; }
    static constexpr bool stamp_at(int st) { return st < 2 || st == NSTG - 2; }
    static constexpr bool drain_at(int st) { return st >= 7 && (st & 3) == 3; }
    static constexpr bool need_bar(int st) { return st < 3 || drain_at(st); }
    static constexpr int nepi(int) { return 45; }
    static constexpr int epi_cost(int, int e) { return e % 5 == 4 ? 4 : 1; }
    static constexpr int epi_vm(int, int e) { return e % 5 == 4; }
    struct Ctx {
        bf16x8 af[2][32]; f32x4 acc[2][9][2]; s16x4 w[1][9][2]; u32x4 eo;
        lds_cptr rb, cb[2];
        unsigned mW, dso, voffW[9], lane16, voffP; rsrc_t rsW, rsA, rsP; unsigned sW, sA[NP], sP[NP];
        Sync y; unsigned rt[NP], s8, slot; unsigned long long pv;
    };
    static constexpr bool poll_at(int st) { return (st & 3) == 2 && st + 2 < NSTG; }
    static constexpr unsigned POLL_NEED = 4;
    template <int P, class X> static DI void pass_end(X&) {}
    template <int ST> static DI unsigned poll_idx(Ctx& c) { return CNT_A + c.rt[(ST + 2) / 4] * 8 + c.s8; }
    template <int ST> static DI void post_barrier(Ctx& c) { if constexpr (drain_at(ST)) signal(c.y, CNT_O + c.rt[ST / 4 - 1], c.slot); }
    template <int G, int I> static DI void dmas(Ctx& c) { dma_s<G * STB + I * 1024, 128 * G * 1152>(c.dso, c.mW, c.sW); }
    template <int G, int I> static DI void dmal(Ctx& c) { dma_l(c.voffW[I], c.rsW, c.dso); }
    template <int G, int S> static DI void readW(Ctx& c, int buf, int cc, int half) { c.w[0][cc][half] = vtr(c.cb[G & 1] + S * 9216 + half * 4608 + cc * 32); }
    template <int G, int I> static DI void setb(Ctx& c) { c.cb[G & 1] = c.rb + (G & 3) * STB; asm volatile("" : "+v"(c.cb[G & 1])); }
    template <int VK, int T> static DI void aload(Ctx& c) { ldfr<(VK & 3) * 1024, true, COH_A>(c.af[T][VK & 31], c.lane16, c.rsA, c.sA[VK >> 4] + (T * 128 + (VK & 12)) * 1024); }
    template <int P, int KS, int CC, int T> static DI void mfma(Ctx& c, const bf16x8& wf) { mf_va<KS == 0>(c.acc[P & 1][CC][T], wf, c.af[T][(P * 16 + KS) & 31]); }
    template <int P, int I> static DI void epi(Ctx& c) {
        constexpr int cc = I / 5, ph = I % 5;
        if constexpr (ph < 4) { const f32x4 x = c.acc[P & 1][cc][ph >> 1]; c.eo[ph] = cvtpk(x[2 * (ph & 1)], x[2 * (ph & 1) + 1]); asm volatile("" : "+v"(c.eo)); }
        else __builtin_amdgcn_raw_buffer_store_b128(c.eo, c.rsP, c.voffP, c.sP[P] + (unsigned)(cc * 128), SC1);
    }
    static DI void run(const unsigned short* W, char* ws, char* lds, const Sync& y, unsigned e, unsigned cg, unsigned s8, Stamp& sp) {
        using E = Eng<CfgO<NP>>; Ctx c; const unsigned wid = y.wid;
        const unsigned lane = fresh_lane(), g = lane >> 4, q = (lane & 15) >> 2, p = lane & 3;
        c.mW = (unsigned)(uintptr_t)lds + wid * 9216; c.lane16 = lane * 16; c.y = y; c.s8 = s8; c.slot = s8 * 4 + cg;
        c.rsW = mkrsrc(W, 4096 * DM * 2); c.rsA = mkrsrc(ws + OFF_CTX, (unsigned)SZ_CTX); c.rsP = mkrsrc(ws + OFF_P1, (unsigned)SZ_PART * 2);
        c.sW = (s8 * 512 + wid * 32) * 1152 + cg * 288;
#pragma unroll
        for (int i = 0; i < NP; ++i) { const unsigned rt = i == NP - 1 ? 8 + e : (e ^ 1) + i; c.rt[i] = rt;
            c.sA[i] = ((rt * 8 + wid * 2) * 128 + s8 * 16) * 1024; c.sP[i] = (rt * 4 + wid) * 294912 + s8 * 4608 + cg * 1152; }
        c.voffP = ((lane & 15) >> 1) * 36864 + g * 32 + (lane & 1) * 16;
#pragma unroll
        for (int b = 0; b < 9; ++b) { const unsigned ci = 64 * b + lane; c.voffW[b] = (ci / 18) * 1152 + (ci % 18) * 16; }
        c.rb = (lds_cptr)lds + (4 * g + q) * 288 + 8 * p;
        E::prologueW(c);
        await(c.y, CNT_A + c.rt[0] * 8 + s8, 4);
        STAMP_AT(sp);
        E::prologueA(c); E::run(c, sp);
        STAMP_AT(sp);
        E::template epi_all<NP - 1>(c);
        barrier_all();
        signal(c.y, CNT_O + c.rt[NP - 1], c.slot);
    }
};

DI void ld8(f32x4& a, f32x4& b, const u32x4& w) { a += f32x4{bflo(w[0]), bfhi(w[0]), bflo(w[1]), bfhi(w[1])}; b += f32x4{bflo(w[2]), bfhi(w[2]), bflo(w[3]), bfhi(w[3])}; }
DI f32x4 up4(const u32x2& w) { return f32x4{bflo(w[0]), bfhi(w[0]), bflo(w[1]), bfhi(w[1])}; }
DI float hsum(const f32x4& v) { return (v[0] + v[1]) + (v[2] + v[3]); }
DI float sum_b1to5(float s) { s += dpp<0x4E>(s); s += dpp<0x124>(s); s += dpp<0x128>(s); return swap32_add(swap16_add(s)); }
template <class F> DI void reduce1(F&& publish, char* ws, const unsigned short* x, char* lds, unsigned wid, unsigned rt, unsigned j) {
    const unsigned lane = fresh_lane(), t = wid * 64 + lane, ttp = rt * 4 + (j >> 2), base = ttp * 294912 + (j & 3) * 73728;
    const rsrc_t rsP = mkrsrc(ws + OFF_P1, (unsigned)SZ_PART * 2), rsH = mkrsrc(ws + OFF_H, S_ * DM * 2), rsN = mkrsrc(ws + OFF_HN, (unsigned)SZ_HN);
    const unsigned it2 = 256 + wid * 8 + (lane >> 4) * 2 + (lane & 1), s2 = (lane >> 1) & 7, r0 = (j & 3) * 4 + (lane & 1);
    const unsigned col[2] = {(t >> 1) * 4, (it2 >> 1) * 4};
    u32x4 pp[2][9]; u32x2 res[4][2];
#pragma unroll
    for (int b = 0; b < 2; ++b) {
#pragma unroll
        for (int s = 0; s < 8; ++s) pp[b][s] = __builtin_amdgcn_raw_buffer_load_b128(rsP, t * 16, base + b * 36864 + s * 4608, SC1);
        pp[b][8] = __builtin_amdgcn_raw_buffer_load_b128(rsP, it2 * 16 + s2 * 4608, base + b * 36864, SC1);
    }
#pragma unroll
    for (int u = 0; u < 4; ++u)
#pragma unroll
        for (int i = 0; i < 2; ++i) res[u][i] = *(const u32x2*)(x + (size_t)((ttp * 2 + (u & 1)) * 16 + r0 + 2 * (u >> 1)) * DM + col[i]);
    f32x4 v[4][2] = {};
#pragma unroll
    for (int b = 0; b < 2; ++b) {
#pragma unroll
        for (int s = 0; s < 8; ++s) ld8(v[2 * b][0], v[2 * b + 1][0], pp[b][s]);
        ld8(v[2 * b][1], v[2 * b + 1][1], pp[b][8]);
    }
    LAS f32x4* red = (LAS f32x4*)(lds_cptr)(lds + 147456);
    const bool own2 = s2 == 0;
    f32x4 sm;
#pragma unroll
    for (int u = 0; u < 4; ++u) {
#pragma unroll
        for (int e = 0; e < 4; ++e) { float z = v[u][1][e]; z += dpp<0x4E>(z); z += dpp<0x124>(z); z += dpp<0x128>(z); v[u][1][e] = z; }
#pragma unroll
        for (int i = 0; i < 2; ++i) {
            v[u][i] += up4(res[u][i]);
            const u32x2 hb = {cvtpk(v[u][i][0], v[u][i][1]), cvtpk(v[u][i][2], v[u][i][3])};
            res[u][i] = hb; v[u][i] = up4(hb);
        }
        sm[u] = sum_b1to5(hsum(v[u][0]) + (own2 ? hsum(v[u][1]) : 0.f));
    }
    if (lane < 2) red[wid * 2 + lane] = sm;
    barrier_lds();
    const unsigned b1 = lane & 1;
    { const f32x4 m = ((red[b1] + red[2 + b1]) + (red[4 + b1] + red[6 + b1])) * (1.0f / DM);
#pragma unroll
      for (int u = 0; u < 4; ++u) { v[u][0] -= m[u]; v[u][1] -= m[u]; sm[u] = sum_b1to5(hsum(v[u][0] * v[u][0]) + (own2 ? hsum(v[u][1] * v[u][1]) : 0.f)); } }
    if (lane < 2) red[8 + wid * 2 + lane] = sm;
    barrier_lds();
    const f32x4 q = ((red[8 + b1] + red[10 + b1]) + (red[12 + b1] + red[14 + b1])) * (1.0f / DM);
#pragma unroll
    for (int u = 0; u < 4; ++u) {
        const float rstd = __builtin_amdgcn_rsqf(q[u] + 1e-5f);
#pragma unroll
        for (int i = 0; i < 2; ++i) {
            const unsigned nt = col[i] >> 4, g = (col[i] >> 2) & 3;
            const u32x2 o = {cvtpk(v[u][i][0] * rstd, v[u][i][1] * rstd), cvtpk(v[u][i][2] * rstd, v[u][i][3] * rstd)};
            __builtin_amdgcn_raw_buffer_store_b64(o, rsN, (((ttp * 2 + (u & 1)) * 18 + (nt >> 1)) * 64 + g * 16 + r0 + 2 * (u >> 1)) * 16 + (nt & 1) * 8, 0, SC1);
        }
    }
    publish();
#pragma unroll
    for (int u = 0; u < 4; ++u)
#pragma unroll
        for (int i = 0; i < 2; ++i) __builtin_amdgcn_raw_buffer_store_b64(res[u][i], rsH, (((ttp * 2 + (u & 1)) * 16 + r0 + 2 * (u >> 1)) * DM + col[i]) * 2, 0, SC1);
}
DI void reduce2(char* ws, unsigned short* out, unsigned wid, unsigned rt, unsigned j) {
    const unsigned lane = fresh_lane(), t = wid * 64 + lane, ttp = rt * 4 + (j >> 2), base = ttp * 147456 + (j & 3) * 36864;
    const rsrc_t rsP = mkrsrc(ws + OFF_P2, (unsigned)SZ_PART), rsH = mkrsrc(ws + OFF_H, S_ * DM * 2);
    const unsigned it3 = 512 + wid * 16 + (lane >> 4) * 4 + (lane & 3), s3 = (lane >> 2) & 3, r = (j & 3) * 4 + (lane & 3);
    u32x4 pp[9]; u32x2 res[3][2]; unsigned col[3];
#pragma unroll
    for (int s = 0; s < 4; ++s) { pp[s] = __builtin_amdgcn_raw_buffer_load_b128(rsP, t * 16, base + s * 9216, SC1); pp[4 + s] = __builtin_amdgcn_raw_buffer_load_b128(rsP, t * 16 + 4096, base + s * 9216, SC1); }
    pp[8] = __builtin_amdgcn_raw_buffer_load_b128(rsP, it3 * 16 + s3 * 9216, base, SC1);
    col[0] = (t >> 2) * 4; col[1] = col[0] + 256; col[2] = (it3 >> 2) * 4;
#pragma unroll
    for (int i = 0; i < 3; ++i)
#pragma unroll
        for (int u = 0; u < 2; ++u) res[i][u] = __builtin_amdgcn_raw_buffer_load_b64(rsH, (((ttp * 2 + u) * 16 + r) * DM + col[i]) * 2, 0, SC1);
    f32x4 v[3][2] = {};
#pragma unroll
    for (int s = 0; s < 4; ++s) { ld8(v[0][0], v[0][1], pp[s]); ld8(v[1][0], v[1][1], pp[4 + s]); }
    ld8(v[2][0], v[2][1], pp[8]);
#pragma unroll
    for (int u = 0; u < 2; ++u)
#pragma unroll
        for (int e = 0; e < 4; ++e) { float z = v[2][u][e]; z += dpp<0x124>(z); z += dpp<0x128>(z); v[2][u][e] = z; }
#pragma unroll
    for (int i = 0; i < 3; ++i)
#pragma unroll
        for (int u = 0; u < 2; ++u) { const f32x4 o = v[i][u] + up4(res[i][u]);
            *(u32x2*)(out + (size_t)((ttp * 2 + u) * 16 + r) * DM + col[i]) = u32x2{cvtpk(o[0], o[1]), cvtpk(o[2], o[3])}; }
}

struct CfgQ : CfgBase {
    static constexpr int TT = 4, NKS = 18, NSTG = 6, AD = QPRE, ADX = 8, PRE_VM = 0, POST_PRO_VM = 0, STB = 24576, W0 = 81920; static constexpr bool A_VIA_LDS = false, WB1 = false;
    static constexpr int spass(int st) { return st / 3; }
    static constexpr int sk0(int st) { return 6 * (st % 3); }
    static constexpr int snks(int) { return 6; }
    static constexpr int nt(int) { return 4; }
    static constexpr int kseq(int st, int s) { return st * 6 + s; }
    static constexpr int nb(int) { return 4; }
    static constexpr int ppw(int) { return 6; }
    static constexpr int wissue(int g) { return g < 3 ? -1 : g - 2; }
    static constexpr bool cert_next(int) { return true; }
    static constexpr bool bar_lgkm(int) { return false; }
    static constexpr bool stamp_at(int st) { return st == 0 || st == 3; }
    static constexpr int nepi(int p) { return p == 0 ? 32 + 35 : 32; }
    static constexpr int epi_cost(int, int e) { return e < 32 ? 1 : ((e - 32) % 5 == 0 ? 2 : 4); }
    static constexpr int epi_vm(int, int e) { return e >= 32 && (e - 32) % 5 != 0; }
    struct Ctx {
        bf16x8 af[4][18]; f32x4 acc[2][4][4]; s16x4 w[2][4][2]; u32x4 qo[4][4];
        lds_cptr rb[4], cb[2][4];
        unsigned mW, mK, dso, voffW, lane16; rsrc_t rsW, rsA, rsK, rsV; unsigned sW, sA, sK, sV, dso2; Sync y; unsigned kcnt; unsigned long long pv;
    };
    static constexpr bool poll_at(int st) { return st == 4; }
    static constexpr unsigned POLL_NEED = 8;
    template <int ST> static DI unsigned poll_idx(Ctx& c) { return c.kcnt; }
    static constexpr int epi_stage(int p, int e) { return p == 0 && e >= 32 ? (e >= 62 ? 5 : 4) : 0; }
    template <int G, int I> static DI void dmas(Ctx& c) { dma_s<W0 + (G % 3) * STB + I * 1024, (192 * (G % 3) + 8 * I) * 8192 + (G / 3) * 128>(c.dso, c.mW, c.sW); }
    template <int G, int I> static DI void dmal(Ctx& c) { dma_l(c.voffW, c.rsW, c.dso); }
    template <int G, int S> static DI void readW(Ctx& c, int buf, int cc, int half) { c.w[buf][cc][half] = vtr(c.cb[G & 1][cc] + S * 4096 + half * 2048); }
    template <int G, int I> static DI void setb(Ctx& c) { c.cb[G & 1][I] = c.rb[I] + (G % 3) * STB; asm volatile("" : "+v"(c.cb[G & 1][I])); }
    template <int KS, int T> static DI void aload(Ctx& c) {
        if constexpr (KS >= QPRE) ldfr<(KS & 3) * 1024, (KS < 16), COH_A>(c.af[T][KS], c.lane16, c.rsA, c.sA + (T * 72 + (KS & ~3)) * 1024); }
    template <int P, int KS, int CC, int T> static DI void mfma(Ctx& c, const bf16x8& wf) {
        if constexpr (KS < 16) mf_va<KS == 0>(c.acc[P][CC][T], wf, c.af[T][KS]); else mf_vv<false>(c.acc[P][CC][T], wf, c.af[T][KS]); }
    template <int P, int I> static DI void epi(Ctx& c) {
        if constexpr (I < 32) { constexpr int tt = I >> 3, T = 2 * P + ((I >> 2) & 1), wd = I & 3;
            const f32x4 x = c.acc[P][2 * (T & 1) + (wd >> 1)][tt];
            c.qo[tt][T][wd] = cvtpk(x[2 * (wd & 1)], x[2 * (wd & 1) + 1]); asm volatile("" : "+v"(c.qo[tt][T]));
        } else { constexpr int j = (I - 32) / 5, k = (I - 32) % 5;
            if constexpr (k == 0) dma_s<j * 16384, (j & 3) * 16384>(c.dso2, c.mK, j < 4 ? c.sK : c.sV);
            else asm volatile("buffer_load_dwordx4 %0, %1, %2 offen offset:%3 sc1 lds" ::"v"(c.lane16), "s"(j < 4 ? c.rsK : c.rsV), "s"(c.dso2), "i"((k - 1) * 1024) : "m0", "memory");
        }
    }
};
#define SPLIT 19
namespace k2 {
struct Ctx {
    bf16x8 qf[4][4];
    bf16x8 f8[8];
    u32x4 po[4][4];
    f32x4 O[8][4], L[4];
    bf16x8 ones;
    bf16x8 fr[4];
    f32x4 S[4][4];
    u32x4 P[2][4];
    f32x4 NM[4];
    unsigned orv;
    float e[2][2];
    lds_cptr fb, kb, vb, kbn;
    unsigned mb, lane16;
    unsigned sK, sV;
    int n1, N, sp;
    char* ws; Sync y;
    unsigned dm[2], dso[2]; rsrc_t drs[2];
    unsigned pf, pfo, pw[4][3];
    unsigned hslot;
    int sig;
};
DI unsigned kbase(int n) { return (unsigned)(n & 3) * 16384u; }
DI unsigned vbase(int n) { return 65536u + (unsigned)(n & 3) * 16384u; }
template <bool ISV, int PART> DI void jobp(Ctx& c, int n) {
    if constexpr (PART == 0) { c.dso[ISV] = (ISV ? c.sV : c.sK) + (unsigned)(n < c.n1 ? n : c.sp + n - c.n1) * 16384u; asm volatile("" : "+s"(c.dso[ISV])); }
    if constexpr (PART == 1) { c.dm[ISV] = c.mb + (ISV ? vbase(n) : kbase(n)); asm volatile("" : "+s"(c.dm[ISV])); }
    if constexpr (PART == 2) { c.drs[ISV] = mkrsrc(c.ws + (ISV ? OFF_VF : OFF_KF), n < c.N ? (unsigned)SZ_KF : 0u); asm volatile("" : "+s"(c.drs[ISV])); }
}
template <bool ISV> DI void job(Ctx& c, int n) { jobp<ISV, 0>(c, n); jobp<ISV, 1>(c, n); jobp<ISV, 2>(c, n); }
template <bool ISV, int I> DI void dmaP(Ctx& c) {
    if constexpr (I == 0) asm volatile("s_mov_b32 m0, %0\n\ts_nop 0\n\tbuffer_load_dwordx4 %1, %2, %3 offen sc1 lds" ::"s"(c.dm[ISV]), "v"(c.lane16), "s"(c.drs[ISV]), "s"(c.dso[ISV]) : "m0", "memory");
    else asm volatile("buffer_load_dwordx4 %0, %1, %2 offen offset:%3 sc1 lds" ::"v"(c.lane16), "s"(c.drs[ISV]), "s"(c.dso[ISV]), "i"(I * 1024) : "m0", "memory");
}
DI void warm_next(Ctx& c) {
    u32x4 rs = {c.pw[0][0], c.pw[0][1], c.pw[0][2], 0x00020000u};
    asm volatile("s_nop 4\n\tbuffer_load_dword %0, %1, %2, 0 offen" : "+v"(c.pf) : "v"(c.pfo), "s"(rs) : "memory");
#pragma unroll
    for (int j = 0; j < 3; ++j) { c.pw[0][j] = c.pw[1][j]; c.pw[1][j] = c.pw[2][j]; c.pw[2][j] = c.pw[3][j]; }
    c.pw[3][2] = 0u;
}
DI bf16x8 rdf(lds_cptr b, int i) { return *(const LAS bf16x8*)(b + i * 1024); }
template <int MODE> DI void mf_qk(f32x4& s, const bf16x8& k, const bf16x8& q, const f32x4& nm) {
    if constexpr (MODE == 0) asm volatile("v_mfma_f32_16x16x32_bf16 %0, %1, %2, %0" : "+v"(s) : "v"(k), "a"(q));
    else if constexpr (MODE == 1) asm volatile("v_mfma_f32_16x16x32_bf16 %0, %1, %2, %3" : "=&v"(s) : "v"(k), "a"(q), "v"(nm));
    else asm volatile("v_mfma_f32_16x16x32_bf16 %0, %1, %2, 0" : "=&v"(s) : "v"(k), "a"(q));
}
DI void mf_pv(f32x4& o, const bf16x8& v, const u32x4& p) { asm volatile("v_mfma_f32_16x16x32_bf16 %0, %1, %2, %0" : "+a"(o) : "v"(v), "v"(p)); }
DI void mf_l(f32x4& o, const bf16x8& v, const u32x4& p) { asm volatile("v_mfma_f32_16x16x32_bf16 %0, %1, %2, %0" : "+a"(o) : "a"(v), "v"(p)); }
template <int U, int K> DI void sm_word(Ctx& c) {
    constexpr int TT = K >> 2, W = K & 3;
    const f32x4& s = c.S[2 * U + (W >> 1)][TT];
    c.P[U][TT][W] = cvtpk(__builtin_amdgcn_exp2f(s[2 * (W & 1)]), __builtin_amdgcn_exp2f(s[2 * (W & 1) + 1]));
    asm volatile("" : "+v"(c.P[U][TT]));
}
template <int U, int K, int H> DI void smE(Ctx& c) { constexpr int TT = K >> 2, W = K & 3; c.e[K & 1][H] = __builtin_amdgcn_exp2f(c.S[2 * U + (W >> 1)][TT][2 * (W & 1) + H]); asm volatile("" : "+v"(c.e[K & 1][H])); }
template <int U, int K> DI void smC(Ctx& c) { constexpr int TT = K >> 2, W = K & 3; unsigned w = cvtpk(c.e[K & 1][0], c.e[K & 1][1]); asm volatile("" : "+v"(w)); c.P[U][TT][W] = w; }
template <int U, int K> DI void smR(Ctx& c) { constexpr int TT = K >> 2, W = K & 3; if constexpr (K == 1) c.orv = c.P[U][TT][0] | c.P[U][TT][1]; else c.orv = c.orv | c.P[U][TT][W - 1] | c.P[U][TT][W]; asm volatile("" : "+v"(c.orv)); }
template <int U, int J> DI void smAt(Ctx& c) {
    constexpr int f = J >> 2, sl = J & 3;
    if constexpr (sl == 0 && f >= 1 && f <= 16) smC<U, (f >= 1 ? f - 1 : 0)>(c);
    if constexpr (sl == 1 && f < 16) smE<U, (f < 16 ? f : 0), 0>(c);
    if constexpr (sl == 2 && f < 16) smE<U, (f < 16 ? f : 0), 1>(c);
    if constexpr (sl == 3 && f >= 2 && f < 16 && ((f - 1) & 1)) smR<U, (f >= 2 ? f - 1 : 1)>(c);
    if constexpr (J == 65) smR<U, 15>(c);
    if constexpr (J == 66) { c.orv &= 0x40004000u; asm volatile("" : "+v"(c.orv)); }
}
constexpr int smN(int ts) { return 4 * (16 - 4 * ts) + 3; }
template <int U, int TS, int I> DI void smOp(Ctx& c) {
    constexpr int k = 4 * TS + (I >> 2), sl = I & 3;
    if constexpr (sl == 0 && k < 16) smE<U, (k < 16 ? k : 0), 0>(c);
    if constexpr (sl == 1 && k > 4 * TS) smC<U, (k > 0 ? k - 1 : 0)>(c);
    if constexpr (sl == 2 && k < 16) smE<U, (k < 16 ? k : 0), 1>(c);
    if constexpr ((sl == 3 || (k == 16 && sl == 2)) && k > 4 * TS && ((k - 1) & 1)) { constexpr int kk = k > 0 ? k - 1 : 1, TT = kk >> 2, W = kk & 3;
        if constexpr (kk == 4 * TS + 1) c.orv = c.P[U][TT][0] | c.P[U][TT][1]; else c.orv = c.orv | c.P[U][TT][W - 1] | c.P[U][TT][W];
        if constexpr (k == 16) c.orv &= 0x40004000u;
        asm volatile("" : "+v"(c.orv)); }
}
template <int U, int TS, int LO, int... Is> DI void smOps(Ctx& c, iseq<Is...>) { (smOp<U, TS, LO + Is>(c), ...); }
template <int U, int TS, int G, int GT> DI void smSpread(Ctx& c) {
    constexpr int lo = G * smN(TS) / GT, hi = (G + 1) * smN(TS) / GT;
    smOps<U, TS, lo>(c, mkseq<hi - lo>{});
}
template <int M> DI void zero_acc(Ctx& c) {
    c.O[M >> 2][M & 3] = f32x4{0.f, 0.f, 0.f, 0.f}; asm volatile("" : "+a"(c.O[M >> 2][M & 3]));
    if constexpr ((M & 7) == 7) { c.L[M >> 3] = f32x4{0.f, 0.f, 0.f, 0.f}; asm volatile("" : "+a"(c.L[M >> 3])); }
}
template <int U, int MODE0, int NEXT, int SMU, int JOB, int M> DI void gapQ(Ctx& c, int n, unsigned nb) {
    constexpr int f = M >> 2, tt = M & 3, a = 2 * U + (f >> 2), T = f & 3;
    mf_qk<T != 0 ? 0 : MODE0>(c.S[a][tt], c.fr[f & 3], c.qf[tt][T], c.NM[tt]);
    SBAR();
    if constexpr (tt == 0) {
        if constexpr (f + 3 < 8) c.fr[(f + 3) & 3] = rdf(c.kb, 8 * U + f + 3);
        else if constexpr (NEXT == 0) c.fr[(f + 3) & 3] = rdf(c.vb, 8 * U + f + 3 - 8);
        else if constexpr (NEXT == 1) c.fr[(f + 3) & 3] = rdf(c.kb, 8 + f + 3 - 8);
    }
    if constexpr (SMU >= 0) smAt<(SMU >= 0 ? SMU : 0), M>(c);
    if constexpr (JOB == 1 && tt == 3 && (f & 1)) dmaP<false, (f >> 1)>(c);
    if constexpr (JOB == 2) zero_acc<M>(c);
    if constexpr (JOB == 3 && (M == 7 || M == 23 || M == 31)) jobp<true, (M == 7 ? 0 : (M == 23 ? 1 : 2))>(c, n);
    if constexpr (JOB == 3 && M == 15) { c.kbn = c.fb + nb; asm volatile("" : "+v"(c.kbn)); }
    SBAR();
}
template <int U, int NEXT, int SMU, int SMN, int JOB, int M> DI void gapP(Ctx& c, int n, unsigned nb) {
    constexpr int f = M >> 2, tt = M & 3;
    if constexpr (f < 8) mf_pv(c.O[f][tt], c.fr[f & 3], c.P[U][tt]); else mf_l(c.L[tt], c.ones, c.P[U][tt]);
    SBAR();
    if constexpr (tt == 0 && f < 8) {
        if constexpr (f + 3 < 8) c.fr[(f + 3) & 3] = rdf(c.vb, 8 * U + f + 3);
        else if constexpr (NEXT == 0) c.fr[(f + 3) & 3] = rdf(c.kb, 8 + f + 3 - 8);
        else if constexpr (NEXT == 1) c.fr[(f + 3) & 3] = rdf(c.kbn, f + 3 - 8);
        else if constexpr (NEXT == 2) c.fr[(f + 3) & 3] = rdf(c.vb, 8 + f + 3 - 8);
    }
    if constexpr (SMU >= 0 && SMN == 8) smAt<(SMU >= 0 ? SMU : 0), 32 + M>(c);
    if constexpr (SMU >= 0 && SMN == 16) { smAt<(SMU >= 0 ? SMU : 0), 2 * M>(c); smAt<(SMU >= 0 ? SMU : 0), 2 * M + 1>(c); }
    if constexpr ((JOB & 1) && tt == 3 && (f & 1) && f < 8) dmaP<U == 1, ((f >> 1) & 3)>(c);
    if constexpr ((JOB & 4) && (M == 7 || M == 15 || M == 23)) jobp<false, (M >> 3) % 3>(c, n);
    if constexpr ((JOB & 8) && (tt == 1 || tt == 2) && f < 8) { constexpr int i = 2 * f + tt - 1;
        asm volatile("buffer_load_dwordx4 %0, %1, %2, %3 offen offset:%4 sc1" : "=v"(c.po[i >> 2][i & 3]) : "v"(c.lane16), "s"(c.drs[0]), "s"(nb + (i >> 2) * 4096u), "i"((i & 3) * 1024) : "memory"); }
    if constexpr ((JOB & 2) && M == 35) { c.kb = c.kbn; c.vb = c.fb + nb; asm volatile("" : "+v"(c.vb)); }
    SBAR();
}
template <int NEXT, int U> DI bf16x8 rdnext(Ctx& c, int j) {
    return NEXT == 0 ? rdf(c.vb, 8 * U + j) : (NEXT == 1 ? rdf(c.kb, 8 + j) : (NEXT == 2 ? rdf(c.kbn, j) : rdf(c.vb, 8 + j)));
}
template <int U, int TQ, int SMU, int TS, int G0, int GT, int JOB, bool FT, int M> DI void gapQd(Ctx& c, int n, unsigned nb) {
    constexpr int NT = 4 - TQ, f = M / NT, tt = TQ + M % NT, a = 2 * U + (f >> 2), T = f & 3;
    mf_qk<T != 0 ? 0 : 1>(c.S[a][tt], c.f8[f], c.qf[tt][T], c.NM[tt]);
    SBAR();
    if constexpr (tt == TQ) { if constexpr (f >= 1) c.f8[f - 1] = rdnext<0, U>(c, f - 1); else if constexpr (!FT) c.f8[7] = rdf(c.kb, 8 * U + 7); }
    if constexpr (FT && tt == TQ + 1 && f < 5) c.f8[f + 3] = rdf(c.kb, 8 * U + f + 3);
    smSpread<SMU, TS, G0 + M, GT>(c);
    if constexpr (JOB == 1 && tt == 3 && (f & 1)) dmaP<false, (f >> 1)>(c);
    if constexpr (JOB == 3 && M == 2) job<true>(c, n);
    if constexpr (JOB == 3 && M == 4) { c.kbn = c.fb + nb; asm volatile("" : "+v"(c.kbn)); }
    SBAR();
}
template <int U, int TP, int NEXT, int SMU, int TS, int G0, int GT, int JOB, int M> DI void gapPd(Ctx& c, int n, unsigned nb) {
    constexpr int NT = 4 - TP, f = M / NT, tt = TP + M % NT;
    if constexpr (f < 8) mf_pv(c.O[f][tt], c.f8[f], c.P[U][tt]); else mf_l(c.L[tt], c.ones, c.P[U][tt]);
    SBAR();
    if constexpr (tt == TP && f < 8) { if constexpr (f >= 1) { if constexpr (NEXT >= 0) c.f8[f - 1] = rdnext<(NEXT >= 0 ? NEXT : 0), U>(c, f - 1); } else c.f8[7] = rdf(c.vb, 8 * U + 7); }
    if constexpr (SMU >= 0) smSpread<(SMU >= 0 ? SMU : 0), TS, G0 + M, GT>(c);
    if constexpr ((JOB & 1) && tt == 3 && (f & 1) && f < 8) dmaP<U == 1, ((f >> 1) & 3)>(c);
    if constexpr ((JOB & 2) && M == 9 * NT - 2) job<false>(c, n);
    if constexpr ((JOB & 2) && M == 9 * NT - 1) { c.kb = c.kbn; c.vb = c.fb + nb; asm volatile("" : "+v"(c.vb)); }
    SBAR();
}
template <int U, int TQ, int SMU, int TS, int G0, int GT, int JOB, bool FT, int... Ms> DI void phQd_(Ctx& c, int n, unsigned nb, iseq<Ms...>) { (gapQd<U, TQ, SMU, TS, G0, GT, JOB, FT, Ms>(c, n, nb), ...); }
template <int U, int TP, int NEXT, int SMU, int TS, int G0, int GT, int JOB, int... Ms> DI void phPd_(Ctx& c, int n, unsigned nb, iseq<Ms...>) { (gapPd<U, TP, NEXT, SMU, TS, G0, GT, JOB, Ms>(c, n, nb), ...); }
template <int U, int MODE0, int NEXT, int SMU, int JOB, int... Ms> DI void phQ_(Ctx& c, int n, unsigned nb, iseq<Ms...>) { (gapQ<U, MODE0, NEXT, SMU, JOB, Ms>(c, n, nb), ...); }
template <int U, int NEXT, int SMU, int SMN, int JOB, int... Ms> DI void phP_(Ctx& c, int n, unsigned nb, iseq<Ms...>) { (gapP<U, NEXT, SMU, SMN, JOB, Ms>(c, n, nb), ...); }
template <int U, int MODE0, int NEXT, int SMU, int JOB> DI void phQ(Ctx& c, int n = 0, unsigned nb = 0) { phQ_<U, MODE0, NEXT, SMU, JOB>(c, n, nb, mkseq<32>{}); }
template <int U, int NEXT, int SMU, int SMN, int JOB> DI void phP(Ctx& c, int n = 0, unsigned nb = 0) { phP_<U, NEXT, SMU, SMN, JOB>(c, n, nb, mkseq<36>{}); }
template <int U, int K0, int... Ks> DI void sm_words(Ctx& c, iseq<Ks...>) { (sm_word<U, K0 + Ks>(c), ...); }
template <int U> DI float rowmax(const Ctx& c, int tt) {
    const f32x4 &x = c.S[2 * U][tt], &y = c.S[2 * U + 1][tt];
    return swap32_max(swap16_max(fmaxf(fmaxf(fmaxf(x[0], x[1]), fmaxf(x[2], x[3])), fmaxf(fmaxf(y[0], y[1]), fmaxf(y[2], y[3])))));
}
template <int U, int TT> DI void mask(Ctx& c, int dq) {
    asm volatile("s_nop 7" : "+v"(dq) :: "memory"); SBAR();
#pragma unroll
    for (int a = 2 * U; a < 2 * U + 2; ++a)
#pragma unroll
        for (int e = 0; e < 4; ++e) if (16 * a + e > dq) c.S[a][TT][e] = -__builtin_inff();
    SBAR();
}
template <int U, bool HASO, int TS> DI void rescue(Ctx& c) {
    asm volatile("s_nop 7" ::: "memory"); SBAR();
#pragma unroll
    for (int tt = TS; tt < 4; ++tt) {
        const float dl = fmaxf(rowmax<U>(c, tt) + 7.0f, 0.0f), f = __builtin_amdgcn_exp2f(-dl);
#pragma unroll
        for (int a = 0; a < 4; ++a) if (HASO || (a >> 1) == U) c.S[a][tt] -= dl;
        c.NM[tt] -= dl;
#pragma unroll
        for (int dt = 0; dt < 8; ++dt) { acc_scale(c.O[dt][tt], f); SBAR(); }
        acc_scale(c.L[tt], f); SBAR();
    }
    sm_words<U, 4 * TS>(c, mkseq<16 - 4 * TS>{});
    asm volatile("s_nop 1" ::: "memory");
}
template <int U, bool HASO, int TS = 0> DI void check(Ctx& c) { if (__builtin_expect(__builtin_amdgcn_ballot_w64(c.orv != 0u) != 0ull, 0)) rescue<U, HASO, TS>(c); }
template <int D> DI void iter_diag(Ctx& c, const int n, const int dq) {
    constexpr int GQ = 8 * (4 - D), GP = 9 * (5 - D);
    phQd_<0, D, 1, D - 1, 0, GQ + GP - 1, 1, D == 1>(c, 0, 0u, mkseq<GQ>{});
    mask<0, D>(c, dq);
    phPd_<0, D - 1, 1, 1, D - 1, GQ, GQ + GP - 1, 0>(c, 0, 0u, mkseq<GP - 1>{}); gapPd<0, D - 1, 1, -1, 0, 0, 1, 0, GP - 1>(c, 0, 0u);
    check<1, true, D - 1>(c);
    phQd_<1, D, 0, D, 0, GQ + GP - 2, 3, false>(c, n + 3, D == 3 ? vbase(n + 1) : kbase(n + 2), mkseq<GQ>{});
    mask<1, D>(c, dq);
    wait_vm_bar<12>();
    SBAR();
    phPd_<1, D - 1, 2, 0, D, GQ, GQ + GP - 2, 1>(c, 0, 0u, mkseq<GP - 2>{});
    gapPd<1, D - 1, 2, -1, 0, 0, 1, 2, GP - 2>(c, n + 5, 0u); gapPd<1, D - 1, 2, -1, 0, 0, 1, 2, GP - 1>(c, 0, vbase(n + 1));
    check<0, true, D>(c);
}
template <bool diag> DI void segment(Ctx& c, const int n0, const int NF, const bool first, const int dq, const int pidx, const unsigned pso, f32x2 (&pml)[4], Stamp& sp) {
    c.kb = c.fb + kbase(n0); asm volatile("" : "+v"(c.kb));
#pragma unroll
    for (int f = 0; f < 3; ++f) c.fr[f] = rdf(c.kb, f);
    SBAR();
    phQ<0, 2, 1, -1, 2>(c);
    if (diag && NF == 0) mask<0, 0>(c, dq); else asm volatile("s_nop 7" ::: "memory");
    SBAR();
#pragma unroll
    for (int tt = 0; tt < 4; ++tt) {
        const float m = rowmax<0>(c, tt) + 7.0f;
        c.S[0][tt] -= m; c.S[1][tt] -= m;
        c.NM[tt] = f32x4{-m, -m, -m, -m};
    }
    asm volatile("s_nop 1" : "+v"(c.NM[0]), "+v"(c.NM[1]), "+v"(c.NM[2]), "+v"(c.NM[3]));
    SBAR();
    phQ<1, 1, 2, 0, 0>(c);
    smC<0, 7>(c);
    sm_words<0, 8>(c, mkseq<8>{});
    if (diag && NF == 0) mask<1, 0>(c, dq);
    barrier_lds();
    c.kb = c.fb + kbase(n0 + 1); c.vb = c.fb + vbase(n0); asm volatile("" : "+v"(c.kb), "+v"(c.vb));
#pragma unroll
    for (int f = 0; f < 3; ++f) c.fr[f] = rdf(c.kb, f);
    warm_next(c);
    job<false>(c, n0 + 4);
    SBAR();
    const int ne = n0 + NF - (diag ? 0 : 1);
    int n = n0;
    while (n < ne) {
    if (n > n0) warm_next(c);
    if (!diag && n == 3) signal(c.y, (unsigned)c.sig, 0);
    const int lim = n + 3 < ne ? n + 3 : ne;
    for (; n < lim; ++n) {
        const bool lastq = n + 1 == ne;
        phQ<0, 1, 0, 1, 1>(c);
        if (diag && lastq) mask<0, 0>(c, dq);
        phP<0, 0, 1, 8, 4>(c, n + 5);
        check<1, true>(c);
        phQ<1, 1, 0, 0, 3>(c, n + 3, (!diag && lastq) ? vbase(n + 1) : kbase(n + 2));
        if (diag && lastq) mask<1, 0>(c, dq);
        wait_vm_bar<12>();
        SBAR();
        phP<1, 1, 0, 8, 3>(c, 0, vbase(n + 1));
        check<0, true>(c);
    }
    }
    if constexpr (diag) {
        c.f8[0] = c.fr[0]; c.f8[1] = c.fr[1]; c.f8[2] = c.fr[2];
        iter_diag<1>(c, n, dq); iter_diag<2>(c, n + 1, dq); iter_diag<3>(c, n + 2, dq); n += 3;
        phPd_<0, 3, 3, 1, 3, 0, 9, 1>(c, 0, 0u, mkseq<9>{});
        check<1, false, 3>(c);
    } else {
        phP<0, 2, 1, 16, 1>(c);
        check<1, false>(c);
    }
    job<true>(c, n + 3);
    if constexpr (!diag) STAMP_AT(sp);
    if constexpr (!diag) poll(c.y, (unsigned)pidx, 1);
    if constexpr (!diag) STAMP_AT(sp);
    if (first) wait_vm_bar<12>(); else { barrier_all(); signal(c.y, (unsigned)c.sig, c.hslot); signal(c.y, (unsigned)c.sig + 8, c.hslot); }
    SBAR();
    if constexpr (diag) phPd_<1, 3, -1, -1, 0, 0, 1, 1>(c, 0, 0u, mkseq<9>{});
    else {
        c.drs[0] = mkrsrc(c.ws + OFF_PO, (unsigned)SZ_PO);
#pragma unroll
        for (int tt = 0; tt < 4; ++tt) pml[tt] = __builtin_bit_cast(f32x2, __builtin_amdgcn_raw_buffer_load_b64(c.drs[0], c.lane16 >> 1, pso + 16384 + tt * 512, SC1));
        SBAR();
        phP<1, 3, -1, 0, 8>(c, 0, pso);
        asm volatile("s_waitcnt vmcnt(0)" : "+v"(c.po[0][0]), "+v"(c.po[0][1]), "+v"(c.po[0][2]), "+v"(c.po[0][3]), "+v"(c.po[1][0]), "+v"(c.po[1][1]), "+v"(c.po[1][2]), "+v"(c.po[1][3]) :: "memory");
        asm volatile("" : "+v"(c.po[2][0]), "+v"(c.po[2][1]), "+v"(c.po[2][2]), "+v"(c.po[2][3]), "+v"(c.po[3][0]), "+v"(c.po[3][1]), "+v"(c.po[3][2]), "+v"(c.po[3][3]) :: "memory");
    }
    asm volatile("s_nop 7" ::: "memory"); SBAR();
}
template <int MODE> DI void finish(Ctx& c, rsrc_t rs, unsigned so, const f32x2 (&pml)[4]) {
    float w[4], w2[4];
#pragma unroll
    for (int tt = 0; tt < 4; ++tt) {
        const float m = -c.NM[tt][0], l = acc_rd(c.L[tt][0]);
        if constexpr (MODE == 2) {
            const float M = fmaxf(m, pml[tt][0]), f = __builtin_amdgcn_exp2f(m - M), f2 = __builtin_amdgcn_exp2f(pml[tt][0] - M), rl = __builtin_amdgcn_rcpf(f * l + f2 * pml[tt][1]);
            w[tt] = f * rl; w2[tt] = f2 * rl;
        } else if constexpr (MODE == 0) w[tt] = __builtin_amdgcn_rcpf(l);
        if constexpr (MODE == 1) __builtin_amdgcn_raw_buffer_store_b64(__builtin_bit_cast(u32x2, f32x2{m, l}), rs, c.lane16 >> 1, so + 16384 + tt * 512, SC1);
    }
#pragma unroll
    for (int tt = 0; tt < 4; ++tt)
#pragma unroll
        for (int T = 0; T < 4; ++T) {
            f32x4 x = acc_rd4(c.O[2 * T][tt]), y = acc_rd4(c.O[2 * T + 1][tt]);
            if constexpr (MODE != 1) { x *= w[tt]; y *= w[tt]; }
            if constexpr (MODE == 2) { const u32x4 pq = c.po[tt][T];
                x += f32x4{bflo(pq[0]), bfhi(pq[0]), bflo(pq[1]), bfhi(pq[1])} * w2[tt]; y += f32x4{bflo(pq[2]), bfhi(pq[2]), bflo(pq[3]), bfhi(pq[3])} * w2[tt]; }
            const u32x4 o = {cvtpk(x[0], x[1]), cvtpk(x[2], x[3]), cvtpk(y[0], y[1]), cvtpk(y[2], y[3])};
            __builtin_amdgcn_raw_buffer_store_b128(o, rs, c.lane16, so + (unsigned)(MODE == 1 ? (tt * 4 + T) * 1024 : (tt * 512 + T) * 1024), SC1);
            SBAR();
        }
}
DI void ldq(Ctx& c, rsrc_t rs, unsigned so) {
    unsigned vo = c.lane16 + so; asm volatile("" : "+v"(vo));
#pragma unroll
    for (int tt = 0; tt < 4; ++tt)
        asm volatile("s_nop 4\n\tbuffer_load_dwordx4 %0, %4, %5, 0 offen sc1\n\tbuffer_load_dwordx4 %1, %4, %5, 0 offen offset:1024 sc1\n\tbuffer_load_dwordx4 %2, %4, %5, 0 offen offset:2048 sc1\n\tbuffer_load_dwordx4 %3, %4, %5, 0 offen offset:3072 sc1"
                     : "=&a"(c.qf[tt][0]), "=&a"(c.qf[tt][1]), "=&a"(c.qf[tt][2]), "=&a"(c.qf[tt][3]) : "v"(vo + tt * 4096), "s"(rs) : "memory");
}
DI void q_request(CfgQ::Ctx& qc, const unsigned short* wq, char* lds, unsigned wid) {
    const unsigned lane = fresh_lane(), h = 4 * (blockIdx.x & 7) + (blockIdx.x >> 6), r8 = lane >> 3;
    qc.mW = (unsigned)(uintptr_t)lds + wid * 6144; qc.rsW = mkrsrc(wq, DM * 4096 * 2); qc.sW = wid * 48 * 8192 + h * 256;
    qc.voffW = r8 * 8192 + (((lane & 7) ^ (((r8 >> 1) & 3) << 1)) << 4);
    Eng<CfgQ>::prologueW(qc);
}
DI void phase(CfgQ::Ctx& qc, const unsigned short* wo, const unsigned short* wg, const unsigned short* wu, const unsigned short* wd, char* ws, char* lds, const Sync& y, Stamp& sp) {
    const unsigned wid = y.wid, lane = fresh_lane(), r = lane & 15, g = lane >> 4, q = (lane & 15) >> 2, p = lane & 3;
    const unsigned bid = blockIdx.x, xcd = bid & 7, idx = bid >> 3, h = 4 * xcd + (idx >> 3), pr = (idx >> 1) & 3, odd = idx & 1, big = 7 - pr, hp = h * 4 + pr;
    Ctx c;
    const unsigned lds0 = (unsigned)(uintptr_t)lds;
    c.mb = lds0 + wid * 4096; c.lane16 = lane * 16; c.ws = ws; c.y = y; c.hslot = h & 3;
    c.sK = h * 128 * 4096 + wid * 4096; c.sV = h * 64 * 8192 + wid * 4096;
    const bool whole = pr == 3;
    c.sp = SPLIT; c.n1 = odd ? 4 * (int)pr + 4 : 64; c.N = whole ? (odd ? 16 : 20) : (odd ? 36 - SPLIT : SPLIT);
    const rsrc_t rsC = mkrsrc(ws + OFF_CTX, (unsigned)SZ_CTX), rsQ = mkrsrc(ws + OFF_QS, (unsigned)SZ_QS);
    const unsigned qso = (hp * 4 + wid) * 16384, pso = (hp * 4 + wid) * 18432;
    {
        qc.mK = c.mb; qc.lane16 = c.lane16;
        qc.rsK = mkrsrc(ws + OFF_KF, (unsigned)SZ_KF); qc.sK = c.sK; qc.y = c.y; qc.kcnt = CNT_K + h; qc.rsV = mkrsrc(ws + OFF_VF, (unsigned)SZ_VF); qc.sV = c.sV;
        { const unsigned row = 4 * g + q, f = (row >> 1) & 3;
#pragma unroll
          for (int cc = 0; cc < 4; ++cc) qc.rb[cc] = (lds_cptr)lds + CfgQ::W0 + row * 128 + ((cc ^ f) << 5) + 8 * p; }
        qc.rsA = mkrsrc(ws + OFF_XN, (unsigned)SZ_XN); qc.sA = (((odd ? pr : big) * 16 + wid) * 18) * 1024;
        Eng<CfgQ>::prologueA(qc);
        Eng<CfgQ>::run(qc, sp);
        STAMP_AT(sp);
        barrier_all();
        Eng<CfgQ>::epi_all<1>(qc);
#pragma unroll
        for (int i = 0; i < 16; ++i) { c.qf[i >> 2][i & 3] = __builtin_bit_cast(bf16x8, qc.qo[i >> 2][i & 3]); asm volatile("" : "+a"(c.qf[i >> 2][i & 3])); }
        if (!odd && !whole) {
#pragma unroll
            for (int i = 0; i < 16; ++i) __builtin_amdgcn_raw_buffer_store_b128(qc.qo[i >> 2][i & 3], rsQ, c.lane16, qso + i * 1024, SC1);
        }
    }
    STAMP_AT(sp);
    c.fb = (lds_cptr)lds + lane * 16; asm volatile("" : "+v"(c.fb));
    c.ones = bf16x8{0x3f80, 0x3f80, 0x3f80, 0x3f80, 0x3f80, 0x3f80, 0x3f80, 0x3f80}; asm volatile("" : "+a"(c.ones));
    const int dq = (int)(16 * wid + r) - (int)(4 * g);
    f32x2 pml[4];
    c.pf = 0u; c.pfo = ((bid * 4 + wid) * 64 + lane) * 128;
    { const unsigned short* pp[4] = {wo, wg, wu, wd};
#pragma unroll
      for (int i = 0; i < 4; ++i) { c.pw[i][0] = (unsigned)(uintptr_t)pp[i]; c.pw[i][1] = (unsigned)((uintptr_t)pp[i] >> 32) & 0xffffu; c.pw[i][2] = i ? DM * FF * 2 : DM * 4096 * 2; } }
    const unsigned b0 = odd ? pr : big;
    if (!odd && !whole) {
        c.sig = (int)(CNT_Q + hp);
        segment<false>(c, 0, SPLIT, true, dq, (int)(CNT_P + hp), pso, pml, sp);
        STAMP_AT(sp);
        finish<2>(c, rsC, ((big * 16 + wid) * 128 + h * 4) * 1024, pml);
        STAMP_AT(sp);
    } else {
        for (int sg = 0; sg < 2; ++sg) {
            c.sig = sg ? (int)(CNT_A + pr * 16 + (h >> 2)) : -1;
            segment<true>(c, sg ? c.n1 : 0, sg ? 4 * (int)big - SPLIT : 4 * (int)b0, sg == 0, dq, -1, pso, pml, sp);
            STAMP_AT(sp);
            if (sg == 0) {
                if (!whole) { await(c.y, CNT_Q + hp, 1); ldq(c, rsQ, qso); }
                SBAR();
                finish<0>(c, rsC, ((b0 * 16 + wid) * 128 + h * 4) * 1024, pml);
                wait_vm<16>();
            } else finish<1>(c, mkrsrc(ws + OFF_PO, (unsigned)SZ_PO), pso, pml);
            STAMP_AT(sp);
            if (whole) { STAMP_AT(sp); STAMP_AT(sp); break; }
        }
    }
    barrier_all();
    asm volatile("" :: "v"(c.pf));
    if (odd && !whole) signal(c.y, CNT_P + hp, 0); else { signal(c.y, CNT_A + b0 * 16 + (h >> 2), h & 3); signal(c.y, CNT_A + b0 * 16 + 8 + (h >> 2), h & 3); }
}
}

__global__ __launch_bounds__(256, 1) void k_main(const unsigned short* __restrict__ x, const unsigned short* __restrict__ ckv, const unsigned short* __restrict__ wuk,
                                                 const unsigned short* __restrict__ wuv, const unsigned short* __restrict__ wq, const unsigned short* __restrict__ wo,
                                                 const unsigned short* __restrict__ wg, const unsigned short* __restrict__ wu, const unsigned short* __restrict__ wd,
                                                 char* __restrict__ ws, unsigned short* __restrict__ out) {
    extern __shared__ __attribute__((aligned(16))) char lds[];
    Stamp sp{};
    STAMP_AT(sp);
    const unsigned wid = rfl(threadIdx.x >> 6), bid = blockIdx.x, xcd = bid & 7, idx = bid >> 3;
    const unsigned sk = xcd >> 1, rt = 8 * (xcd & 1) + (idx & 7), cg = idx >> 3;
    const unsigned rt2 = 4 * (xcd & 1) + (idx & 3), cg2 = 8 * (xcd >> 1) + (idx >> 2);
    Sync y{ws, wid, 0ull};
    CfgQ::Ctx qc;
    kv_phase([&] { k2::q_request(qc, wq, lds, wid); }, x, ckv, wuk, wuv, ws, wq, wo, lds, y, (idx & 1) ? ((idx >> 1) & 3) : 7 - ((idx >> 1) & 3), qc.af, sp);
    STAMP_AT(sp);
    k2::phase(qc, wo, wg, wu, wd, ws, lds, y, sp);
    if (idx & 1) CfgO<3>::run(wo, ws, lds, y, idx & 7, cg, xcd, sp); else CfgO<1>::run(wo, ws, lds, y, idx & 7, cg, xcd, sp);
    {
        using C = CfgGU; using E = Eng<C>; C::Ctx c;
        C::setup(c, wg, wu, ws, lds, wid, rt2, cg2);
        E::prologueWr<0, 2>(c);
        STAMP_AT(sp);
        await(y, CNT_O + rt, 32);
        STAMP_AT(sp);
        reduce1([&] { barrier_all(); signal(y, CNT_H + (rt >> 1), (rt & 1) * 16 + sk * 4 + cg); }, ws, x, lds, wid, rt, sk * 4 + cg);
        E::prologueWr<2, C::NSTG>(c);
        STAMP_AT(sp);
        await(y, CNT_H + rt2, 32);
        STAMP_AT(sp);
        E::prologueA(c); E::run(c, sp);
        STAMP_AT(sp);
        C::tail(c, cg2 & 1);
        barrier_all();
        signal(y, CNT_G + rt2 * 4 + (cg2 >> 3), cg2 & 7);
    }
    {
        using C = CfgN<18>; using E = Eng<C>; C::Ctx c;
        C::setup(c, wd, ws + OFF_ACT, (unsigned)SZ_ACT, ws + OFF_P2, lds, wid, rt, cg, sk);
        E::prologueW(c);
        STAMP_AT(sp);
        await(y, CNT_G + (rt >> 1) * 4 + sk, 8);
        STAMP_AT(sp);
        E::prologueA(c); E::run(c, sp);
        STAMP_AT(sp);
        C::tail(c, lds, wid);
        barrier_all();
        signal(y, CNT_D + rt, sk * 4 + cg);
        STAMP_AT(sp);
        await(y, CNT_D + rt, 16);
        STAMP_AT(sp);
        reduce2(ws, out, wid, rt, sk * 4 + cg);
        if (bid == 0) signal(y, CNT_N, 0);
    }
    STAMP_AT(sp);
    STAMP_OUT(sp, ws, 0);
}

extern "C" __global__ void k_last() {}

extern "C" void kernel_launch(void* const* d_in, const int* in_sizes, int n_in, void* d_out, int out_size, void* d_ws, size_t ws_size, hipStream_t stream) {
    const unsigned short* x = (const unsigned short*)d_in[0];
    const unsigned short* ckv = (const unsigned short*)d_in[1];
    const unsigned short* wq = (const unsigned short*)d_in[2];
    const unsigned short* wuk = (const unsigned short*)d_in[3];
    const unsigned short* wuv = (const unsigned short*)d_in[4];
    const unsigned short* wo = (const unsigned short*)d_in[5];
    const unsigned short* wg = (const unsigned short*)d_in[7];
    const unsigned short* wu = (const unsigned short*)d_in[8];
    const unsigned short* wd = (const unsigned short*)d_in[9];
    char* ws = (char*)d_ws;
    (void)hipFuncSetAttribute((const void*)k_main, hipFuncAttributeMaxDynamicSharedMemorySize, 163840);
    k_main<<<256, 256, 163840, stream>>>(x, ckv, wuk, wuv, wq, wo, wg, wu, wd, ws, (unsigned short*)d_out);
}
```
